```python
import torch

def _softmax(x, axis=-1):
    return torch.softmax(x, dim=axis)
def _gelu(x):
    return torch.nn.functional.gelu(x, approximate="tanh")
def _silu(x):
    return torch.nn.functional.silu(x)
def _sigmoid(x):
    return torch.sigmoid(x)
def _layernorm(x, axis=-1, eps=1e-5):
    m = x.mean(dim=axis, keepdim=True)
    v = x.var(dim=axis, keepdim=True, unbiased=False)
    return (x - m) / torch.sqrt(v + eps)
def _scatter_add(tgt, idx, src, axis=0):
    return tgt.clone().index_add_(axis, idx, src)
def _segment_sum(src, seg, n):
    out = torch.zeros((n,) + src.shape[1:], dtype=src.dtype)
    return out.index_add_(0, seg, src)
def _pad(x, pw):
    flat = tuple(v for pair in reversed(pw) for v in pair)
    return torch.nn.functional.pad(x, flat)

kernel_name = 'concat_heads__gqa_block_cached__gqa_block_cached__near_ridge'

def setup_inputs():

    _w = (lambda a: (lambda t: t.to(torch.bfloat16) if t.is_floating_point() else t)(torch.as_tensor(a)))
    def _A_setup():
        B, Sq, D = 16, 64, 448
        Hq, Hkv, Sk, Dh = 16, 8, 1024, 256
        assert Hq % Hkv == 0, (Hq, Hkv)
        return {
            "x": _w((lambda *s: torch.randn(*s, dtype=torch.bfloat16) * 0.8)(B, Sq, D)),
            "k_cache": _w((lambda *s: torch.randn(*s, dtype=torch.bfloat16) * 0.8)(B, Hkv, Sk, Dh)),
            "v_cache": _w((lambda *s: torch.randn(*s, dtype=torch.bfloat16) * 0.05)(B, Hkv, Sk, Dh)),
            "wq": _w((lambda *s: torch.randn(*s, dtype=torch.bfloat16) * 0.8)(D, Hq * Dh) * (D ** -0.5)),
            "wo": _w((lambda *s: torch.randn(*s, dtype=torch.bfloat16) * 0.05)(Hq * Dh, D) * ((Hq * Dh) ** -0.5)),
        }
    _A_in = _A_setup()

    def _B_setup():
        B, Sq, D = 16, 64, 448
        Hq, Hkv, Sk, Dh = 16, 8, 1024, 256
        assert Hq % Hkv == 0, (Hq, Hkv)
        return {
            "x": _w((lambda *s: torch.randn(*s, dtype=torch.bfloat16) * 0.8)(B, Sq, D)),
            "k_cache": _w((lambda *s: torch.randn(*s, dtype=torch.bfloat16) * 0.8)(B, Hkv, Sk, Dh)),
            "v_cache": _w((lambda *s: torch.randn(*s, dtype=torch.bfloat16) * 0.05)(B, Hkv, Sk, Dh)),
            "wq": _w((lambda *s: torch.randn(*s, dtype=torch.bfloat16) * 0.8)(D, Hq * Dh) * (D ** -0.5)),
            "wo": _w((lambda *s: torch.randn(*s, dtype=torch.bfloat16) * 0.05)(Hq * Dh, D) * ((Hq * Dh) ** -0.5)),
        }
    _B_in = _B_setup()

    return {
        "A_x": _A_in["x"],
        "A_k_cache": _A_in["k_cache"],
        "A_v_cache": _A_in["v_cache"],
        "A_wq": _A_in["wq"],
        "A_wo": _A_in["wo"],
        "B_x": _B_in["x"],
        "B_k_cache": _B_in["k_cache"],
        "B_v_cache": _B_in["v_cache"],
        "B_wq": _B_in["wq"],
        "B_wo": _B_in["wo"],
    }

def reference(A_x, A_k_cache, A_v_cache, A_wq, A_wo, B_x, B_k_cache, B_v_cache, B_wq, B_wo):
    def _A_ref(x, k_cache, v_cache, wq, wo):
        B, Sq, D = x.shape
        Hq, Dh = 16, 256
        Hkv = k_cache.shape[1]
        rep = Hq // Hkv
        q = (lambda x, *d: x.permute(*d))((x @ wq).reshape(B, Sq, Hq, Dh), 0, 2, 1, 3)
        k = (lambda x, n, axis: torch.repeat_interleave(x, n, dim=axis))(k_cache, rep, axis=1)
        v = (lambda x, n, axis: torch.repeat_interleave(x, n, dim=axis))(v_cache, rep, axis=1)
        scores = torch.einsum("bhqd,bhkd->bhqk", q, k) * (Dh ** -0.5)
        p = _softmax(scores, axis=-1)
        o = torch.einsum("bhqk,bhkd->bhqd", p, v)
        return (lambda x, *d: x.permute(*d))(o, 0, 2, 1, 3).reshape(B, Sq, Hq * Dh) @ wo

    def _B_ref(x, k_cache, v_cache, wq, wo):
        B, Sq, D = x.shape
        Hq, Dh = 16, 256
        Hkv = k_cache.shape[1]
        rep = Hq // Hkv
        q = (lambda x, *d: x.permute(*d))((x @ wq).reshape(B, Sq, Hq, Dh), 0, 2, 1, 3)
        k = (lambda x, n, axis: torch.repeat_interleave(x, n, dim=axis))(k_cache, rep, axis=1)
        v = (lambda x, n, axis: torch.repeat_interleave(x, n, dim=axis))(v_cache, rep, axis=1)
        scores = torch.einsum("bhqd,bhkd->bhqk", q, k) * (Dh ** -0.5)
        p = _softmax(scores, axis=-1)
        o = torch.einsum("bhqk,bhkd->bhqd", p, v)
        return (lambda x, *d: x.permute(*d))(o, 0, 2, 1, 3).reshape(B, Sq, Hq * Dh) @ wo

    a = _A_ref(A_x, A_k_cache, A_v_cache, A_wq, A_wo)
    b = _B_ref(B_x, B_k_cache, B_v_cache, B_wq, B_wo)
    return torch.concatenate([a, b], axis=-1)
```

```cpp
#include <hip/hip_runtime.h>
#include <hip/hip_bf16.h>
#include <stdint.h>

typedef __attribute__((ext_vector_type(8))) short bf16x8;
typedef __attribute__((ext_vector_type(4))) short s16x4;
typedef __attribute__((ext_vector_type(4))) float f32x4;
typedef __attribute__((ext_vector_type(2))) float f32x2;
typedef __attribute__((ext_vector_type(2))) __bf16 bf16x2_t;
typedef __attribute__((ext_vector_type(4))) unsigned u32x4;
typedef __attribute__((ext_vector_type(2))) unsigned u32x2;
#define LAS __attribute__((address_space(3)))
typedef LAS const char* lds_cptr;
typedef LAS char* lds_ptr;

constexpr int NB = 16, SQ = 64, DM = 448, HQ = 16, HKV = 8, SK = 1024, DH = 256;
constexpr int SS = 32768;
constexpr int LDS_BYTES = 5 * SS;
constexpr float CEXP = 0.0625f * 1.4426950408889634f;

constexpr size_t WS_PART = 65536;

#define MFMA16(a, b, c) __builtin_amdgcn_mfma_f32_16x16x32_bf16((a), (b), (c), 0, 0, 0)
#define WAIT_BAR(N) asm volatile("s_waitcnt vmcnt(" #N ") lgkmcnt(0)\n\ts_barrier" ::: "memory")
#define LGKM_BAR() asm volatile("s_waitcnt lgkmcnt(0)\n\ts_barrier" ::: "memory")
template <int N> __device__ __forceinline__ void wait_bar() { asm volatile("s_waitcnt vmcnt(%0) lgkmcnt(0)\n\ts_barrier" ::"i"(N) : "memory"); }
template <int N> __device__ __forceinline__ void wait_vm() { asm volatile("s_waitcnt vmcnt(%0)" ::"i"(N) : "memory"); }

__device__ __forceinline__ unsigned cvt2(float a, float b) {
  f32x2 v = {a, b};
  return __builtin_bit_cast(unsigned, __builtin_convertvector(v, bf16x2_t));
}
__device__ __forceinline__ bf16x8 ldsb128(lds_cptr p) { return *(const LAS bf16x8*)p; }
__device__ __forceinline__ s16x4 ldstr(lds_cptr p) { return __builtin_amdgcn_ds_read_tr16_b64_v4i16((LAS s16x4*)p); }
__device__ __forceinline__ bf16x8 cat8(s16x4 a, s16x4 b) { return __builtin_shufflevector(a, b, 0, 1, 2, 3, 4, 5, 6, 7); }

template <bool NT> __device__ __forceinline__ void dma4(unsigned lds, const void* sbase, unsigned v0, unsigned v1, unsigned v2, unsigned v3) {
  unsigned keep;
  if constexpr (NT)
    asm volatile("s_mov_b32 %0, m0\n\ts_mov_b32 m0, %1\n\ts_nop 3\n\t"
                 "global_load_lds_dwordx4 %2, %6 nt\n\tglobal_load_lds_dwordx4 %3, %6 offset:1024 nt\n\t"
                 "global_load_lds_dwordx4 %4, %6 offset:2048 nt\n\tglobal_load_lds_dwordx4 %5, %6 offset:3072 nt\n\ts_mov_b32 m0, %0"
                 : "=&s"(keep) : "s"(lds), "v"(v0), "v"(v1), "v"(v2), "v"(v3), "s"(sbase) : "memory");
  else
    asm volatile("s_mov_b32 %0, m0\n\ts_mov_b32 m0, %1\n\ts_nop 3\n\t"
                 "global_load_lds_dwordx4 %2, %6\n\tglobal_load_lds_dwordx4 %3, %6 offset:1024\n\t"
                 "global_load_lds_dwordx4 %4, %6 offset:2048\n\tglobal_load_lds_dwordx4 %5, %6 offset:3072\n\ts_mov_b32 m0, %0"
                 : "=&s"(keep) : "s"(lds), "v"(v0), "v"(v1), "v"(v2), "v"(v3), "s"(sbase) : "memory");
}
template <bool NT> __device__ __forceinline__ void dma1(unsigned lds, const void* sbase, unsigned v0) {
  unsigned keep;
  if constexpr (NT)
    asm volatile("s_mov_b32 %0, m0\n\ts_mov_b32 m0, %1\n\ts_nop 3\n\tglobal_load_lds_dwordx4 %2, %3 nt\n\ts_mov_b32 m0, %0"
                 : "=&s"(keep) : "s"(lds), "v"(v0), "s"(sbase) : "memory");
  else
    asm volatile("s_mov_b32 %0, m0\n\ts_mov_b32 m0, %1\n\ts_nop 3\n\tglobal_load_lds_dwordx4 %2, %3\n\ts_mov_b32 m0, %0"
                 : "=&s"(keep) : "s"(lds), "v"(v0), "s"(sbase) : "memory");
}

#define W_NT true

__global__ __launch_bounds__(512, 2) void gqa_fused_kernel(
    const char* __restrict__ xA, const char* __restrict__ kA, const char* __restrict__ vA, const char* __restrict__ wqA, const char* __restrict__ woA,
    const char* __restrict__ xB, const char* __restrict__ kB, const char* __restrict__ vB, const char* __restrict__ wqB, const char* __restrict__ woB,
    char* __restrict__ out, char* __restrict__ ws) {
  extern __shared__ __attribute__((aligned(16))) char smem[];
  const lds_ptr L = (lds_ptr)smem;
  const unsigned lds0 = (unsigned)(uintptr_t)L;
  const int tid = threadIdx.x, lane = tid & 63;
  const int w = __builtin_amdgcn_readfirstlane(tid >> 6);
  const int g = lane >> 4, li = lane & 15, qq = li >> 2, pp = li & 3;
  const int id = blockIdx.x, kvh = id & 7, grp = id >> 3, br = grp >> 4, b = grp & 15;

  const char* xg = (br ? xB : xA) + (size_t)b * (SQ * DM * 2);
  const char* kg = (br ? kB : kA) + (size_t)(b * HKV + kvh) * (SK * DH * 2);
  const char* vg = (br ? vB : vA) + (size_t)(b * HKV + kvh) * (SK * DH * 2);
  const char* wqg = (br ? wqB : wqA) + (size_t)kvh * 1024;
  const char* wog = (br ? woB : woA) + (size_t)kvh * 512 * (DM * 2);

  unsigned kofs[4], vofs[4];
#pragma unroll
  for (int j = 0; j < 4; ++j) {
    const int row = 8 * w + 2 * j + (lane >> 5), cp = lane & 31;
    kofs[j] = row * 512 + ((cp ^ (row & 15)) << 4) - 1024 * j;
    vofs[j] = row * 512 + ((cp ^ ((row & 7) << 1)) << 4) - 1024 * j;
  }

  const int rot = b % 14;
  unsigned wofs[4];
  if (w < 4) {
    for (int i = 0; i < 14; ++i) {
      const int piece = w * 14 + i, P = piece * 64 + lane, row = P / 56, cp = P - row * 56;
      dma1<W_NT>(lds0 + SS + piece * 1024, xg, row * 896 + ((cp ^ ((row >> 1) & 7)) << 4));
    }
#pragma unroll
    for (int j = 0; j < 4; ++j) {
      const int fw = (j | ((w & 1) << 2)) << 1;
      wofs[j] = (8 * w + j) * 8192 + ((lane ^ fw) << 4) - 1024 * j;
    }
#pragma unroll
    for (int i = 0; i < 2; ++i) {
      int ks = i + rot; ks = ks >= 14 ? ks - 14 : ks;
      const char* src = wqg + (size_t)ks * (32 * 8192);
      dma4<W_NT>(lds0 + (3 + i) * SS + w * 8192, src, wofs[0], wofs[1], wofs[2], wofs[3]);
      dma4<W_NT>(lds0 + (3 + i) * SS + w * 8192 + 4096, src + 4 * 8192, wofs[0], wofs[1], wofs[2], wofs[3]);
    }
    wait_vm<16>();
  } else {
    dma4<true>(lds0 + w * 4096, kg, kofs[0], kofs[1], kofs[2], kofs[3]);
    dma4<true>(lds0 + (w - 4) * 4096, kg - 4 * 4096, kofs[0], kofs[1], kofs[2], kofs[3]);
  }
  LGKM_BAR();
  const int mh = w >> 2, nq = w & 3;
  bf16x8 xf[14][2];
#pragma unroll
  for (int i = 0; i < 14; ++i) {
    int ks = i + rot; ks = ks >= 14 ? ks - 14 : ks;
#pragma unroll
    for (int mb = 0; mb < 2; ++mb) {
      const int row = 32 * mh + 16 * mb + li, c = 4 * ks + g;
      xf[i][mb] = ldsb128(L + SS + row * 896 + ((c ^ ((row >> 1) & 7)) << 4));
    }
  }
  LGKM_BAR();
  if (w >= 4) {
    dma4<true>(lds0 + SS + w * 4096, vg, vofs[0], vofs[1], vofs[2], vofs[3]);
    dma4<true>(lds0 + SS + (w - 4) * 4096, vg - 4 * 4096, vofs[0], vofs[1], vofs[2], vofs[3]);
    dma4<true>(lds0 + 2 * SS + w * 4096, kg + SS, kofs[0], kofs[1], kofs[2], kofs[3]);
    dma4<true>(lds0 + 2 * SS + (w - 4) * 4096, kg + SS - 4 * 4096, kofs[0], kofs[1], kofs[2], kofs[3]);
  }
  f32x4 qa[2][4][2];
#pragma unroll
  for (int mb = 0; mb < 2; ++mb)
#pragma unroll
    for (int s = 0; s < 4; ++s) { qa[mb][s][0] = f32x4{0.f, 0.f, 0.f, 0.f}; qa[mb][s][1] = f32x4{0.f, 0.f, 0.f, 0.f}; }
  {
    const int fw = (qq | ((g & 1) << 2)) << 1;
    const unsigned wbase = 3 * SS + (8 * g + qq) * 1024 + (((16 * nq + pp) ^ fw) << 4) + (pp >> 1) * 8;
#pragma unroll
    for (int i = 0; i < 14; ++i) {
      if (w < 4) { if (i < 13) wait_vm<8>(); else wait_vm<0>(); }
      LGKM_BAR();
      bf16x8 wf[4][2];
#pragma unroll
      for (int s = 0; s < 4; ++s)
#pragma unroll
        for (int blk = 0; blk < 2; ++blk) {
          const lds_cptr p = L + (wbase ^ (unsigned)((s << 6) | (blk << 3))) + (i & 1) * SS;
          wf[s][blk] = cat8(ldstr(p), ldstr(p + 4096));
        }
      LGKM_BAR();
      if (i + 2 < 14 && w < 4) {
        int ks = i + 2 + rot; ks = ks >= 14 ? ks - 14 : ks;
        const char* src = wqg + (size_t)ks * (32 * 8192);
        dma4<W_NT>(lds0 + (3 + (i & 1)) * SS + w * 8192, src, wofs[0], wofs[1], wofs[2], wofs[3]);
        dma4<W_NT>(lds0 + (3 + (i & 1)) * SS + w * 8192 + 4096, src + 4 * 8192, wofs[0], wofs[1], wofs[2], wofs[3]);
      }
#pragma unroll
      for (int s = 0; s < 4; ++s)
#pragma unroll
        for (int blk = 0; blk < 2; ++blk)
#pragma unroll
          for (int mb = 0; mb < 2; ++mb) qa[mb][s][blk] = MFMA16(wf[s][blk], xf[i][mb], qa[mb][s][blk]);
    }
  }
#pragma unroll
  for (int mb = 0; mb < 2; ++mb)
#pragma unroll
    for (int s = 0; s < 4; ++s) {
      const unsigned x0 = cvt2(qa[mb][s][0][0], qa[mb][s][0][1]), x1 = cvt2(qa[mb][s][0][2], qa[mb][s][0][3]);
      const unsigned y0 = cvt2(qa[mb][s][1][0], qa[mb][s][1][1]), y1 = cvt2(qa[mb][s][1][2], qa[mb][s][1][3]);
      const bool lo = g < 2;
      u32x4 f = {lo ? x0 : y0, lo ? x1 : y1, lo ? y0 : x0, lo ? y1 : x1};
      const int cw = (nq >> 1) * 4 + mh * 2 + mb, kst = (nq & 1) * 4 + s;
      *(LAS u32x4*)(L + 3 * SS + cw * 8192 + kst * 1024 + lane * 16) = f;
    }
  LGKM_BAR();
  bf16x8 qf[8];
#pragma unroll
  for (int s = 0; s < 8; ++s) qf[s] = ldsb128(L + 3 * SS + w * 8192 + s * 1024 + lane * 16);
  WAIT_BAR(0);
  dma4<true>(lds0 + 3 * SS + w * 4096, vg + SS, vofs[0], vofs[1], vofs[2], vofs[3]);
  dma4<true>(lds0 + 4 * SS + w * 4096, kg + 2 * SS, kofs[0], kofs[1], kofs[2], kofs[3]);

  const int rot7 = b % 7;
  unsigned oofs[4];
#pragma unroll
  for (int j = 0; j < 4; ++j) {
    const int r8 = lane >> 3, cp = lane & 7;
    oofs[j] = (32 * w + 8 * j + r8) * 896 + ((cp ^ (((r8 >> 1) & 3) << 1)) << 4) - 1024 * j;
  }
  auto wo_src = [&](int T) { int r = (T >> 1) + rot7; r = r >= 7 ? r - 7 : r; return wog + (size_t)(T & 1) * (256 * 896) + r * 128; };

  f32x4 oa[16];
#pragma unroll
  for (int d = 0; d < 16; ++d) oa[d] = f32x4{0.f, 0.f, 0.f, 0.f};
  float m_run = -INFINITY, l_run = 0.f;
  const unsigned kbase = li * 512 + ((g ^ li) << 4);
  const unsigned vbase = (4 * g + qq) * 512 + ((((4 * (g & 1) + qq) << 1) | (pp >> 1)) << 4) + (pp & 1) * 8;
  int slot = 0;
  for (int t = 0; t < 16; ++t) {
    WAIT_BAR(12);
    {
      const int fs = slot == 0 ? 4 * SS : slot - SS;
      if (t >= 1) {
        if (t < 14) dma4<true>(lds0 + fs + w * 4096, kg + (size_t)(t + 2) * SS, kofs[0], kofs[1], kofs[2], kofs[3]);
        else dma4<W_NT>(lds0 + fs + w * 4096, wo_src(2 * (t - 14)), oofs[0], oofs[1], oofs[2], oofs[3]);
      }
    }
    f32x4 sa[4];
#pragma unroll
    for (int kb = 0; kb < 4; ++kb) sa[kb] = f32x4{0.f, 0.f, 0.f, 0.f};
#pragma unroll
    for (int s = 0; s < 8; ++s) {
      const lds_cptr p = L + slot + (kbase ^ (unsigned)((s & 3) << 6)) + (s >> 2) * 256;
#pragma unroll
      for (int kb = 0; kb < 4; ++kb) sa[kb] = MFMA16(ldsb128(p + kb * 8192), qf[s], sa[kb]);
    }
    slot = slot == 4 * SS ? 0 : slot + SS;
    float mx = fmaxf(fmaxf(sa[0][0], sa[0][1]), fmaxf(sa[0][2], sa[0][3]));
#pragma unroll
    for (int kb = 1; kb < 4; ++kb) mx = fmaxf(mx, fmaxf(fmaxf(sa[kb][0], sa[kb][1]), fmaxf(sa[kb][2], sa[kb][3])));
    { auto r = __builtin_amdgcn_permlane16_swap(__float_as_uint(mx), __float_as_uint(mx), false, false); mx = fmaxf(__uint_as_float(r[0]), __uint_as_float(r[1])); }
    { auto r = __builtin_amdgcn_permlane32_swap(__float_as_uint(mx), __float_as_uint(mx), false, false); mx = fmaxf(__uint_as_float(r[0]), __uint_as_float(r[1])); }
    const float m_new = fmaxf(m_run, mx * CEXP);
    const float alpha = __builtin_amdgcn_exp2f(m_run - m_new);
    m_run = m_new;
    float ps = 0.f;
#pragma unroll
    for (int kb = 0; kb < 4; ++kb)
#pragma unroll
      for (int r = 0; r < 4; ++r) { const float p = __builtin_amdgcn_exp2f(fmaf(sa[kb][r], CEXP, -m_new)); sa[kb][r] = p; ps += p; }
    l_run = l_run * alpha + ps;
    bf16x8 pf[2];
#pragma unroll
    for (int ks = 0; ks < 2; ++ks) {
      u32x4 f = {cvt2(sa[2 * ks][0], sa[2 * ks][1]), cvt2(sa[2 * ks][2], sa[2 * ks][3]), cvt2(sa[2 * ks + 1][0], sa[2 * ks + 1][1]), cvt2(sa[2 * ks + 1][2], sa[2 * ks + 1][3])};
      pf[ks] = __builtin_bit_cast(bf16x8, f);
    }
#pragma unroll
    for (int d = 0; d < 16; ++d) oa[d] *= alpha;
    WAIT_BAR(12);
    {
      const int fs = slot == 0 ? 4 * SS : slot - SS;
      if (t < 14) dma4<true>(lds0 + fs + w * 4096, vg + (size_t)(t + 2) * SS, vofs[0], vofs[1], vofs[2], vofs[3]);
      else if (t == 14) dma4<W_NT>(lds0 + fs + w * 4096, wo_src(1), oofs[0], oofs[1], oofs[2], oofs[3]);
    }
#pragma unroll
    for (int d = 0; d < 16; ++d) {
      const lds_cptr p = L + slot + (vbase ^ (unsigned)((d & 7) << 5)) + (d >> 3) * 256;
#pragma unroll
      for (int ks = 0; ks < 2; ++ks) oa[d] = MFMA16(cat8(ldstr(p + ks * 16384), ldstr(p + ks * 16384 + 8192)), pf[ks], oa[d]);
    }
    slot = slot == 4 * SS ? 0 : slot + SS;
  }
  { auto r = __builtin_amdgcn_permlane16_swap(__float_as_uint(l_run), __float_as_uint(l_run), false, false); l_run = __uint_as_float(r[0]) + __uint_as_float(r[1]); }
  { auto r = __builtin_amdgcn_permlane32_swap(__float_as_uint(l_run), __float_as_uint(l_run), false, false); l_run = __uint_as_float(r[0]) + __uint_as_float(r[1]); }
  const float inv = 1.0f / l_run;
  LGKM_BAR();
#pragma unroll
  for (int s = 0; s < 8; ++s) {
    u32x4 f = {cvt2(oa[2 * s][0] * inv, oa[2 * s][1] * inv), cvt2(oa[2 * s][2] * inv, oa[2 * s][3] * inv),
               cvt2(oa[2 * s + 1][0] * inv, oa[2 * s + 1][1] * inv), cvt2(oa[2 * s + 1][2] * inv, oa[2 * s + 1][3] * inv)};
    *(LAS u32x4*)(L + w * 8192 + s * 1024 + lane * 16) = f;
  }
  LGKM_BAR();
  const int qh = w >> 2;
  bf16x8 of[2][2][8];
#pragma unroll
  for (int hh = 0; hh < 2; ++hh)
#pragma unroll
    for (int qb = 0; qb < 2; ++qb)
#pragma unroll
      for (int s = 0; s < 8; ++s) of[hh][qb][s] = ldsb128(L + (hh * 4 + qh * 2 + qb) * 8192 + s * 1024 + lane * 16);
  LGKM_BAR();
  dma4<W_NT>(lds0 + 0 * SS + w * 4096, wo_src(3), oofs[0], oofs[1], oofs[2], oofs[3]);
  dma4<W_NT>(lds0 + 1 * SS + w * 4096, wo_src(4), oofs[0], oofs[1], oofs[2], oofs[3]);

  const __amdgpu_buffer_rsrc_t wsr = __builtin_amdgcn_make_buffer_rsrc(ws, 0, 0x7fffffff, 0x00020000);
  const unsigned obase = (4 * g + qq) * 128 + (((2 * nq + (pp >> 1)) ^ ((((4 * g + qq) >> 1) & 3) << 1)) << 4) + (pp & 1) * 8;
  unsigned long long* ctl = (unsigned long long*)(ws + (size_t)grp * 2048);
  unsigned long long ep = 0;
  f32x4 pa[2];
#pragma unroll
  for (int T = 0; T < 14; ++T) {
    if (T == 0) wait_bar<16>(); else if (T == 1) wait_bar<12>(); else if (T <= 3) wait_bar<13>(); else if (T <= 10) wait_bar<14>();
    else if (T == 11) wait_bar<10>(); else if (T == 12) wait_bar<6>(); else wait_bar<2>();
    if (T >= 1 && T + 4 < 14) dma4<W_NT>(lds0 + ((T + 1) % 5) * SS + w * 4096, wo_src(T + 4), oofs[0], oofs[1], oofs[2], oofs[3]);
    if (T == 13) ep = __hip_atomic_load(ctl, __ATOMIC_RELAXED, __HIP_MEMORY_SCOPE_AGENT);
    if ((T & 1) == 0) { pa[0] = f32x4{0.f, 0.f, 0.f, 0.f}; pa[1] = f32x4{0.f, 0.f, 0.f, 0.f}; }
    const lds_cptr p = L + ((T + 2) % 5) * SS + obase;
#pragma unroll
    for (int s = 0; s < 8; ++s) {
      const bf16x8 a = cat8(ldstr(p + s * 4096), ldstr(p + s * 4096 + 2048));
      pa[0] = MFMA16(a, of[T & 1][0][s], pa[0]);
      pa[1] = MFMA16(a, of[T & 1][1][s], pa[1]);
    }
    if (T & 1) {
      int r = (T >> 1) + rot7; r = r >= 7 ? r - 7 : r;
      u32x4 f = {cvt2(pa[0][0], pa[0][1]), cvt2(pa[0][2], pa[0][3]), cvt2(pa[1][0], pa[1][1]), cvt2(pa[1][2], pa[1][3])};
      const unsigned off = (unsigned)WS_PART + (((unsigned)(grp * 56 + r * 8 + w) * 8 + kvh) << 10) + lane * 16;
      __builtin_amdgcn_raw_buffer_store_b128(f, wsr, off, 0, 16);
    }
  }
  const unsigned long long tag = ep * 0x9E3779B97F4A7C15ull + 0x632BE59BD9B4E019ull;
  WAIT_BAR(0);
  if (tid == 0) __hip_atomic_store(ctl + 16 * (kvh + 1), tag, __ATOMIC_RELAXED, __HIP_MEMORY_SCOPE_AGENT);
  if (w < 7) {
    for (unsigned spin = 0; spin < (1u << 22); ++spin) {
      unsigned long long f = tag;
      if (lane < 8) f = __hip_atomic_load(ctl + 16 * (lane + 1), __ATOMIC_RELAXED, __HIP_MEMORY_SCOPE_AGENT);
      if (__all(f == tag)) break;
      __builtin_amdgcn_s_sleep(2);
    }
    __builtin_amdgcn_fence(__ATOMIC_ACQUIRE, "wavefront");
    const int pc = kvh * 7 + w, r = pc >> 3, pw = pc & 7;
    const unsigned off = (unsigned)WS_PART + ((unsigned)(grp * 56 + pc) << 13) + lane * 16;
    u32x4 v[8];
#pragma unroll
    for (int s = 0; s < 8; ++s) v[s] = __builtin_amdgcn_raw_buffer_load_b128(wsr, off + s * 1024, 0, 16);
    float acc[8];
#pragma unroll
    for (int e = 0; e < 8; ++e) acc[e] = 0.f;
#pragma unroll
    for (int s = 0; s < 8; ++s)
#pragma unroll
      for (int e = 0; e < 4; ++e) { acc[2 * e] += __uint_as_float(v[s][e] << 16); acc[2 * e + 1] += __uint_as_float(v[s][e] & 0xffff0000u); }
    const int q = 32 * (pw >> 2) + li, n = 64 * r + 16 * (pw & 3) + 4 * g;
    char* o = out + ((size_t)(b * SQ + q) * (2 * DM) + br * DM + n) * 2;
    *(u32x2*)o = u32x2{cvt2(acc[0], acc[1]), cvt2(acc[2], acc[3])};
    *(u32x2*)(o + 16 * 2 * DM * 2) = u32x2{cvt2(acc[4], acc[5]), cvt2(acc[6], acc[7])};
    if (kvh == 0 && tid == 0) __hip_atomic_store(ctl, tag, __ATOMIC_RELAXED, __HIP_MEMORY_SCOPE_AGENT);
  }
}

extern "C" void kernel_launch(void* const* d_in, const int* in_sizes, int n_in, void* d_out, int out_size, void* d_ws, size_t ws_size,
                              hipStream_t stream) {
  (void)in_sizes; (void)n_in; (void)out_size; (void)ws_size;
  (void)hipFuncSetAttribute((const void*)gqa_fused_kernel, hipFuncAttributeMaxDynamicSharedMemorySize, LDS_BYTES);
  gqa_fused_kernel<<<256, 512, LDS_BYTES, stream>>>(
      (const char*)d_in[0], (const char*)d_in[1], (const char*)d_in[2], (const char*)d_in[3], (const char*)d_in[4],
      (const char*)d_in[5], (const char*)d_in[6], (const char*)d_in[7], (const char*)d_in[8], (const char*)d_in[9],
      (char*)d_out, (char*)d_ws);
}
```

```cpp
#include <hip/hip_runtime.h>
#include <hip/hip_bf16.h>
#include <stdint.h>
#include <type_traits>

typedef __attribute__((ext_vector_type(8))) short bf16x8;
typedef __attribute__((ext_vector_type(4))) short s16x4;
typedef __attribute__((ext_vector_type(4))) float f32x4;
typedef __attribute__((ext_vector_type(16))) float f32x16;
typedef __attribute__((ext_vector_type(2))) float f32x2;
typedef __attribute__((ext_vector_type(2))) __bf16 bf16x2_t;
typedef __attribute__((ext_vector_type(4))) unsigned u32x4;
typedef __attribute__((ext_vector_type(2))) unsigned u32x2;
#define LAS __attribute__((address_space(3)))
typedef LAS const char* lds_cptr;
typedef LAS char* lds_ptr;

constexpr int SQ = 64, DM = 448, HKV = 8, SK = 1024, DH = 256;
constexpr int SS = 32768;
constexpr int MISC = 4 * SS;
constexpr int LDS_BYTES = 5 * SS;
constexpr int XRING = 3 * SS;
constexpr int QST = 3 * SS;
constexpr float CEXP = 0.0625f * 1.4426950408889634f;
#define VDEPTH 5
#define THR 60.0f

constexpr size_t WS_PART = 65536;

#define MFMA16(a, b, c) __builtin_amdgcn_mfma_f32_16x16x32_bf16((a), (b), (c), 0, 0, 0)
#define MFMA32(a, b, c) __builtin_amdgcn_mfma_f32_32x32x16_bf16((a), (b), (c), 0, 0, 0)
#define CBAR() asm volatile("s_waitcnt lgkmcnt(0)\n\ts_barrier" ::: "memory")
#define SBAR() __builtin_amdgcn_sched_barrier(0)
#define OPAQUE(x) asm volatile("" : "+v"(x))
#define DI __device__ __forceinline__
template <int N> DI void wbar() { asm volatile("s_waitcnt vmcnt(%0) lgkmcnt(0)\n\ts_barrier" ::"i"(N) : "memory"); }
DI void bar() { asm volatile("s_barrier" ::: "memory"); }

DI unsigned cvt2(float a, float b) { f32x2 v = {a, b}; return __builtin_bit_cast(unsigned, __builtin_convertvector(v, bf16x2_t)); }
DI bf16x8 ldsb128(unsigned a) { return *(const LAS bf16x8*)(uintptr_t)a; }
DI s16x4 ldstr(unsigned a) { return __builtin_amdgcn_ds_read_tr16_b64_v4i16((LAS s16x4*)(uintptr_t)a); }
DI bf16x8 cat8(s16x4 a, s16x4 b) { return __builtin_shufflevector(a, b, 0, 1, 2, 3, 4, 5, 6, 7); }
DI int lane_id() { int l; asm volatile("v_mbcnt_lo_u32_b32 %0, -1, 0\n\tv_mbcnt_hi_u32_b32 %0, -1, %0" : "=v"(l)); return l; }
DI float xsum(float v) {
  { auto r = __builtin_amdgcn_permlane16_swap(__float_as_uint(v), __float_as_uint(v), false, false); v = __uint_as_float(r[0]) + __uint_as_float(r[1]); }
  { auto r = __builtin_amdgcn_permlane32_swap(__float_as_uint(v), __float_as_uint(v), false, false); v = __uint_as_float(r[0]) + __uint_as_float(r[1]); }
  return v;
}
DI float xmax(float v) {
  { auto r = __builtin_amdgcn_permlane16_swap(__float_as_uint(v), __float_as_uint(v), false, false); v = fmaxf(__uint_as_float(r[0]), __uint_as_float(r[1])); }
  { auto r = __builtin_amdgcn_permlane32_swap(__float_as_uint(v), __float_as_uint(v), false, false); v = fmaxf(__uint_as_float(r[0]), __uint_as_float(r[1])); }
  return v;
}

DI void dma4(unsigned lds, const void* sbase, unsigned v0, unsigned v1, unsigned v2, unsigned v3) {
  unsigned keep;
  asm volatile("s_mov_b32 %0, m0\n\ts_mov_b32 m0, %1\n\ts_nop 3\n\t"
               "global_load_lds_dwordx4 %2, %6 nt\n\tglobal_load_lds_dwordx4 %3, %6 offset:1024 nt\n\t"
               "global_load_lds_dwordx4 %4, %6 offset:2048 nt\n\tglobal_load_lds_dwordx4 %5, %6 offset:3072 nt\n\ts_mov_b32 m0, %0"
               : "=&s"(keep) : "s"(lds), "v"(v0), "v"(v1), "v"(v2), "v"(v3), "s"(sbase) : "memory");
}

DI void dma2(unsigned lds, const void* sbase, unsigned v0, unsigned v1) {
  unsigned keep;
  asm volatile("s_mov_b32 %0, m0\n\ts_mov_b32 m0, %1\n\ts_nop 3\n\t"
               "global_load_lds_dwordx4 %2, %4 nt\n\tglobal_load_lds_dwordx4 %3, %4 offset:1024 nt\n\ts_mov_b32 m0, %0"
               : "=&s"(keep) : "s"(lds), "v"(v0), "v"(v1), "s"(sbase) : "memory");
}
DI void dma3(unsigned lds, const void* sbase, unsigned v0, unsigned v1, unsigned v2) {
  unsigned keep;
  asm volatile("s_mov_b32 %0, m0\n\ts_mov_b32 m0, %1\n\ts_nop 3\n\t"
               "global_load_lds_dwordx4 %2, %5 nt\n\tglobal_load_lds_dwordx4 %3, %5 offset:1024 nt\n\t"
               "global_load_lds_dwordx4 %4, %5 offset:2048 nt\n\ts_mov_b32 m0, %0"
               : "=&s"(keep) : "s"(lds), "v"(v0), "v"(v1), "v"(v2), "s"(sbase) : "memory");
}

#define STAMP(i) do {} while (0)
#define GSTAMP(i) do {} while (0)

__global__ __launch_bounds__(512, 2) void gqa_fused_kernel(
    const char* __restrict__ xA, const char* __restrict__ kA, const char* __restrict__ vA, const char* __restrict__ wqA, const char* __restrict__ woA,
    const char* __restrict__ xB, const char* __restrict__ kB, const char* __restrict__ vB, const char* __restrict__ wqB, const char* __restrict__ woB,
    char* __restrict__ out, char* __restrict__ ws) {
  extern __shared__ __attribute__((aligned(16))) char smem[];
  const unsigned lds0 = (unsigned)(uintptr_t)(lds_ptr)smem;
  int lane = threadIdx.x & 63;
  const int w = __builtin_amdgcn_readfirstlane(threadIdx.x >> 6);
  const int id = blockIdx.x, kvh = id & 7, grp = id >> 3, br = grp >> 4, b = grp & 15;
  const int rot7 = b % 7;
  const int rg = w & 3, hf = w >> 2;
  STAMP(0);
  const char* kg = (br ? kB : kA) + (size_t)(b * HKV + kvh) * (SK * DH * 2);
  const char* vg = (br ? vB : vA) + (size_t)(b * HKV + kvh) * (SK * DH * 2);
  const char* wog = (br ? woB : woA) + (size_t)kvh * 512 * (DM * 2);
  int g = lane >> 4, li = lane & 15, qq = li >> 2, pp = li & 3;

  auto k_eighth = [&](unsigned dst, const char* src, int e) {
    const int ln = lane_id();
    const int hi = ln >> 5, cp = ln & 31, r0 = 8 * (e & 1) + hi;
    const unsigned base = hi * 512;
    dma4(dst + e * 4096, src + e * 4096, base + ((cp ^ r0) << 4), base + ((cp ^ (r0 + 2)) << 4), base + ((cp ^ (r0 + 4)) << 4), base + ((cp ^ (r0 + 6)) << 4));
  };

  if (w >= 4) {
    const int lw = w - 4;
    const char* xg = (br ? xB : xA) + (size_t)b * (SQ * DM * 2);
    const char* wqg = (br ? wqB : wqA) + (size_t)kvh * 1024;
    unsigned wofs[4];
#pragma unroll
    for (int j = 0; j < 4; ++j) wofs[j] = j * (8192 - 1024) + ((lane ^ (j << 2)) << 4);
    int wsl = 1;
    auto wtile = [&](int i) {
      int G = (i >> 1) + rot7; G = G >= 7 ? G - 7 : G;
      const char* src = wqg + (size_t)(G * 64 + (i & 1) * 32 + 8 * lw) * 8192;
      const unsigned dst = lds0 + wsl * SS + lw * 8192;
      dma4(dst, src, wofs[0], wofs[1], wofs[2], wofs[3]);
      dma4(dst + 4096, src + 4 * 8192, wofs[0], wofs[1], wofs[2], wofs[3]);
      wsl = wsl == 2 ? 0 : wsl + 1;
    };
    auto ktile = [&](int t) { k_eighth(lds0 + t * SS, kg + (size_t)t * SS, 2 * lw); k_eighth(lds0 + t * SS, kg + (size_t)t * SS, 2 * lw + 1); };
    const int xr = 16 * lw + (lane >> 3), xc = lane & 7;
    const unsigned x0 = xr * 896 + ((xc ^ ((xr >> 1) & 7)) << 4), x1 = (xr + 8) * 896 + ((xc ^ (((xr + 8) >> 1) & 7)) << 4) - 1024;
    auto xtile = [&](int Gi) { int G = Gi + rot7; G = G >= 7 ? G - 7 : G; dma2(lds0 + XRING + Gi * 8192 + lw * 2048, xg + G * 128, x0, x1); };
    xtile(0); wtile(0);
    for (int Gi = 1; Gi < 7; ++Gi) xtile(Gi);
    wtile(1);
    for (int i = 0; i < 14; ++i) {
      if (i == 0) wbar<20>(); else wbar<8>();
      if (i + 2 < 14) wtile(i + 2); else ktile(i - 12);
    }
    bar();
  } else {
    const int mh = w & 1, hh = w >> 1, h = lane >> 5;
    f32x16 qa[8];
#pragma unroll
    for (int a = 0; a < 8; ++a) qa[a] = (f32x16)(0.f);
    const unsigned wbase = lds0 + (8 * h + qq) * 1024 + hh * 512 + (((2 * (g & 1) + (pp >> 1)) ^ (qq << 2)) << 4) + (pp & 1) * 8;
    const int m = lane & 31;
    const unsigned xbase = lds0 + XRING + (32 * mh + m) * 128 + ((h ^ ((m >> 1) & 7)) << 4);
    bf16x8 wfA[8], wfB[8], xa, xb, xo;
#pragma unroll
    for (int a = 0; a < 8; ++a) wfB[a] = (bf16x8)(0);
    xo = (bf16x8)(0);
    int sl = SS;
    for (int i = 0; i < 14; ++i) {
      CBAR();
      unsigned p = wbase + sl; OPAQUE(p);
      sl = sl == 2 * SS ? 0 : sl + SS;
      const unsigned xp = (xbase + (i >> 1) * 8192) ^ (unsigned)((i & 1) << 6);
      xa = ldsb128(xp); xb = ldsb128(xp ^ 32);
      SBAR();
#pragma unroll
      for (int a = 0; a < 8; ++a) {
        const unsigned q = (p ^ (unsigned)((a & 3) << 6)) + (a >> 2) * 256; wfA[a] = cat8(ldstr(q), ldstr(q + 4096));
        qa[a] = MFMA32(wfB[a], xo, qa[a]);
        SBAR();
      }
#pragma unroll
      for (int a = 0; a < 8; ++a) {
        if (a < 4) {
#pragma unroll
          for (int u = 0; u < 2; ++u) { const int a2 = 2 * a + u; const unsigned q = (p ^ (unsigned)((a2 & 3) << 6)) + (a2 >> 2) * 256; wfB[a2] = cat8(ldstr(q + 16384), ldstr(q + 16384 + 4096)); }
        }
        qa[a] = MFMA32(wfA[a], xa, qa[a]);
        SBAR();
      }
      xo = xb;
    }
    CBAR();
    const unsigned qw = lds0 + QST + ((w >> 1) * 2 + mh) * 16384 + (m >> 4) * 8192 + (m & 15) * 16 + h * 8;
#pragma unroll
    for (int nb = 0; nb < 8; ++nb) {
      qa[nb] = MFMA32(wfB[nb], xo, qa[nb]);
#pragma unroll
      for (int a = 0; a < 4; ++a)
        *(LAS u32x2*)(uintptr_t)(qw + nb * 1024 + a * 256) = u32x2{cvt2(qa[nb][4 * a], qa[nb][4 * a + 1]), cvt2(qa[nb][4 * a + 2], qa[nb][4 * a + 3])};
    }
  }
  CBAR();
  bf16x8 qf[2][8];
#pragma unroll
  for (int qb = 0; qb < 2; ++qb)
#pragma unroll
    for (int s = 0; s < 8; ++s) qf[qb][s] = ldsb128(lds0 + QST + ((rg * 2 + qb) * 8 + s) * 1024 + lane * 16);
  STAMP(1);

  const int vrot = id & 3;
  auto issue_vw = [&](int p) {
    const unsigned dst = lds0 + (p & 3) * SS + w * 4096;
    const int ln = lane_id();
    const bool isv = p < 20 || (p < 44 && !(p & 1));
    if (isv) {
      const int n = p < 20 ? p - 16 : 4 + ((p - 20) >> 1);
      const int r8 = ln >> 3, cp = ln & 7;
      const unsigned v0 = r8 * 512 + ((cp ^ (((r8 >> 1) & 3) << 1)) << 4);
      dma4(dst, vg + (size_t)((n & 3) * 256 + 32 * w) * 512 + (((n >> 2) + vrot) & 3) * 128, v0, v0 + 3072, v0 + 6144, v0 + 9216);
    } else {
      const int k = p < 44 ? (p - 20) >> 1 : p - 32, wv = w < 7 ? w : 6;
      unsigned o[4];
#pragma unroll
      for (int i = 0; i < 4; ++i) { const int P0 = wv * 256 + ln, row = ((P0 + 64 * i) * 4682) >> 18; o[i] = (P0 ^ (((row >> 1) & 3) << 1)) << 4; }
      dma4(dst, wog + (size_t)(((k >> 1) & 1) * 256 + (((k >> 2) + vrot) & 3) * 64 + (k & 1) * 32) * 896, o[0], o[1], o[2], o[3]);
    }
  };
  auto issue = [&](int p) { if (p < 16) k_eighth(lds0 + (p & 3) * SS, kg + (size_t)p * SS, w); else issue_vw(p); };

  u32x4 pst[16][2];
  float mref[2] = {-INFINITY, -INFINITY}, lsum[2], tmax[2];
  const unsigned kbase = lds0 + hf * 16384 + li * 512 + ((g ^ li) << 4);
  for (int attempt = 0;; ++attempt) {
    lsum[0] = lsum[1] = 0.f; tmax[0] = tmax[1] = -INFINITY;
    if (hf == 1) wbar<8>();
#pragma unroll
    for (int t = 0; t < 16; ++t) {
      if (hf == 0) { if (t < 2) CBAR(); else wbar<4>(); }
      f32x4 sa[2][2];
      {
        bf16x8 kf[3][2];
        unsigned kb0 = kbase + (t & 3) * SS; OPAQUE(kb0);
        auto kread = [&](int s) { const unsigned p = (kb0 ^ (unsigned)((s & 3) << 6)) + (s >> 2) * 256; kf[s % 3][0] = ldsb128(p); kf[s % 3][1] = ldsb128(p + 8192); };
        kread(0); kread(1); kread(2); SBAR();
#pragma unroll
        for (int s = 0; s < 8; ++s) {
#pragma unroll
          for (int kb = 0; kb < 2; ++kb)
#pragma unroll
            for (int qb = 0; qb < 2; ++qb) sa[kb][qb] = MFMA16(kf[s % 3][kb], qf[qb][s], s == 0 ? (f32x4)(0.f) : sa[kb][qb]);
          SBAR();
          if (s + 3 < 8) { kread(s + 3); SBAR(); }
        }
      }
      issue(t + 2);
      if (hf == 1) wbar<4>();
#pragma unroll
      for (int qb = 0; qb < 2; ++qb) {
        const float mx = fmaxf(fmaxf(fmaxf(sa[0][qb][0], sa[0][qb][1]), fmaxf(sa[0][qb][2], sa[0][qb][3])), fmaxf(fmaxf(sa[1][qb][0], sa[1][qb][1]), fmaxf(sa[1][qb][2], sa[1][qb][3])));
        tmax[qb] = fmaxf(tmax[qb], mx);
        if (t == 0) mref[qb] = fmaxf(mref[qb], xmax(mx) * CEXP);
        float e[8];
#pragma unroll
        for (int r = 0; r < 8; ++r) { e[r] = __builtin_amdgcn_exp2f(fmaf(sa[r >> 2][qb][r & 3], CEXP, -mref[qb])); lsum[qb] += e[r]; }
        pst[t][qb] = u32x4{cvt2(e[0], e[1]), cvt2(e[2], e[3]), cvt2(e[4], e[5]), cvt2(e[6], e[7])};
        OPAQUE(pst[t][qb]); OPAQUE(lsum[qb]); OPAQUE(tmax[qb]);
      }
      SBAR();
    }
    float bad = 0.f;
#pragma unroll
    for (int qb = 0; qb < 2; ++qb) { tmax[qb] = xmax(tmax[qb]) * CEXP; lsum[qb] = xsum(lsum[qb]); if (!(tmax[qb] - mref[qb] <= THR)) bad = 1.f; }
    const float anybad = __any(bad != 0.f) ? 1.f : 0.f;
    if (g == 0) *(LAS f32x4*)(uintptr_t)(lds0 + MISC + w * 512 + li * 16) = f32x4{mref[0], lsum[0], mref[1], lsum[1]};
    if (lane == 0) *(LAS float*)(uintptr_t)(lds0 + MISC + 4096 + w * 4) = anybad;
    if (hf == 0) wbar<4>();
    CBAR();
    const f32x4 f0 = *(const LAS f32x4*)(uintptr_t)(lds0 + MISC + 4096), f1 = *(const LAS f32x4*)(uintptr_t)(lds0 + MISC + 4096 + 16);
    const float fl = (f0[0] + f0[1]) + (f0[2] + f0[3]) + (f1[0] + f1[1]) + (f1[2] + f1[3]);
    if (attempt == 1 || __builtin_amdgcn_readfirstlane(__float_as_uint(fl)) == 0u) break;
    mref[0] = tmax[0]; mref[1] = tmax[1];
    wbar<0>();
    issue(0); issue(1);
    wbar<0>();
  }
  float cown[2];
  {
    const f32x4 o = *(const LAS f32x4*)(uintptr_t)(lds0 + MISC + (w ^ 4) * 512 + li * 16);
#pragma unroll
    for (int qb = 0; qb < 2; ++qb) {
      const float mo = o[2 * qb], lo = o[2 * qb + 1], m = fmaxf(mref[qb], mo);
      const float ea = __builtin_amdgcn_exp2f(mref[qb] - m), eb = __builtin_amdgcn_exp2f(mo - m);
      cown[qb] = ea / (lsum[qb] * ea + lo * eb);
    }
  }
  float cpk = (g & 1) ? cown[1] : cown[0];
  OPAQUE(cpk);
  STAMP(2);

  const int qh = w >> 2, nq = w & 3;
  f32x4 pa[7][2], oa[4][2];
#pragma unroll
  for (int i = 0; i < 7; ++i) { pa[i][0] = (f32x4)(0.f); pa[i][1] = (f32x4)(0.f); }

  auto vtile = [&](int slot, auto cc) {
    constexpr int c = decltype(cc)::value;
    const int ln = lane_id(), rl = ((ln >> 4) << 2) | ((ln >> 2) & 3);
    const unsigned vb = lds0 + slot * SS + hf * 4096 + rl * 128 + ((((ln >> 1) & 1) ^ (((rl >> 1) & 3) << 1) ^ (hf << 2)) << 4) + (ln & 1) * 8;
    constexpr int VD = VDEPTH;
    s16x4 vt[VD][2];
    auto vread = [&](int f) {
      const unsigned a = (vb ^ (unsigned)((f & 3) << 5)) + (f >> 2) * 8192; vt[f % VD][0] = ldstr(a); vt[f % VD][1] = ldstr(a + 2048);
    };
#pragma unroll
    for (int f = 0; f < VD; ++f) vread(f);
    SBAR();
#pragma unroll
    for (int f = 0; f < 16; ++f) {
      const bf16x8 a = cat8(vt[f % VD][0], vt[f % VD][1]);
#pragma unroll
      for (int qb = 0; qb < 2; ++qb)
        oa[f & 3][qb] = MFMA16(a, __builtin_bit_cast(bf16x8, pst[4 * c + (f >> 2)][qb]), (c == 0 && f < 4) ? (f32x4)(0.f) : oa[f & 3][qb]);
      SBAR();
      if (f + VD < 16) { vread(f + VD); SBAR(); }
    }
  };
  bf16x8 o0, o1;
  auto oload = [&](int idx) {
    const unsigned oad = lds0 + MISC + ((((idx >> 1) * 2 + qh) + 4 * ((idx & 1) ^ 1)) * 4096) + lane_id() * 16;
    o0 = ldsb128(oad); o1 = ldsb128(oad + 1024);
  };
  auto wtile2 = [&](int slot) {
    const int ln = lane_id();
    const int rl = ((ln >> 4) << 2) | ((ln >> 2) & 3);
    const unsigned wb = lds0 + slot * SS + rl * 896 + (ln & 1) * 8;
    const unsigned cx = ((ln >> 1) & 1) ^ (((rl >> 1) & 3) << 1);
    s16x4 at[3][2];
    auto aread = [&](int i) { const unsigned a = wb + (((unsigned)(14 * nq + 2 * i) ^ cx) << 4); at[i % 3][0] = ldstr(a); at[i % 3][1] = ldstr(a + 16 * 896); };
    aread(0); aread(1); aread(2); SBAR();
#pragma unroll
    for (int i = 0; i < 7; ++i) {
      const bf16x8 a = cat8(at[i % 3][0], at[i % 3][1]);
      pa[i][0] = MFMA16(a, o0, pa[i][0]); pa[i][1] = MFMA16(a, o1, pa[i][1]);
      SBAR();
      if (i + 3 < 7) { aread(i + 3); SBAR(); }
    }
  };
  auto comb_write = [&]() {
    const int ln = lane_id();
    float cc = cpk; OPAQUE(cc);
    const auto cr = __builtin_amdgcn_permlane16_swap(__float_as_uint(cc), __float_as_uint(cc), false, false);
    const float c0 = __uint_as_float(cr[0]), c1 = __uint_as_float(cr[1]);
#pragma unroll
    for (int db = 0; db < 4; ++db) { oa[db][0] *= c0; oa[db][1] *= c1; }
#pragma unroll
    for (int k = 0; k < 2; ++k)
#pragma unroll
      for (int qb = 0; qb < 2; ++qb) {
        const f32x4 v = oa[2 + k][qb];
        *(LAS f32x4*)(uintptr_t)(lds0 + MISC + w * 4096 + (k * 2 + qb) * 1024 + ln * 16) = v;
      }
  };
  auto comb_read = [&]() {
    const int ln = lane_id();
    const unsigned pad = lds0 + MISC + (w ^ 4) * 4096 + ln * 16;
    u32x4 f[2];
#pragma unroll
    for (int qb = 0; qb < 2; ++qb) {
      const f32x4 a = *(const LAS f32x4*)(uintptr_t)(pad + qb * 1024) + oa[0][qb];
      const f32x4 c = *(const LAS f32x4*)(uintptr_t)(pad + (2 + qb) * 1024) + oa[1][qb];
      f[qb] = u32x4{cvt2(a[0], a[1]), cvt2(a[2], a[3]), cvt2(c[0], c[1]), cvt2(c[2], c[3])};
    }
    *(LAS u32x4*)(uintptr_t)(pad) = f[0];
    *(LAS u32x4*)(uintptr_t)(pad + 1024) = f[1];
    SBAR();
  };
  using I0 = std::integral_constant<int, 0>; using I1 = std::integral_constant<int, 1>; using I2 = std::integral_constant<int, 2>; using I3 = std::integral_constant<int, 3>;
  issue_vw(18); vtile(0, I0{}); issue_vw(19);
  wbar<8>(); vtile(1, I1{}); issue_vw(20);
  wbar<8>(); vtile(2, I2{}); issue_vw(21);
  wbar<8>(); vtile(3, I3{}); comb_write(); issue_vw(22);
#pragma unroll 1
  for (int p = 20; p < 44; p += 8) {
    wbar<8>(); comb_read(); vtile(0, I0{}); issue_vw(p + 3);
    wbar<8>(); oload(0); wtile2(1); issue_vw(p + 4);
    wbar<8>(); vtile(2, I1{}); issue_vw(p + 5);
    wbar<8>(); oload(1); wtile2(3); issue_vw(p + 6);
    wbar<8>(); vtile(0, I2{}); issue_vw(p + 7);
    wbar<8>(); oload(2); wtile2(1); issue_vw(p + 8);
    wbar<8>(); vtile(2, I3{}); oload(3); issue_vw(p + 9);
    wbar<8>(); comb_write(); wtile2(3); issue_vw(p + 10);
  }
  STAMP(3);
  CBAR(); comb_read(); issue_vw(47);
  wbar<8>(); oload(0); wtile2(0); oload(1); wtile2(1);
  wbar<0>();
  unsigned long long* ctl = (unsigned long long*)(ws + (size_t)grp * 2048);
  const unsigned long long ep = __hip_atomic_load(ctl, __ATOMIC_RELAXED, __HIP_MEMORY_SCOPE_AGENT);
  oload(2); wtile2(2); oload(3); wtile2(3);
  lane = lane_id(); g = lane >> 4; li = lane & 15;
  const __amdgpu_buffer_rsrc_t wsr = __builtin_amdgcn_make_buffer_rsrc(ws, 0, 0x7fffffff, 0x00020000);
#pragma unroll
  for (int i = 0; i < 7; ++i) {
    u32x4 f = {cvt2(pa[i][0][0], pa[i][0][1]), cvt2(pa[i][0][2], pa[i][0][3]), cvt2(pa[i][1][0], pa[i][1][1]), cvt2(pa[i][1][2], pa[i][1][3])};
    const unsigned off = (unsigned)WS_PART + (((unsigned)(grp * 56 + w * 7 + i) * 8 + kvh) << 10) + lane * 16;
    __builtin_amdgcn_raw_buffer_store_b128(f, wsr, off, 0, 16);
  }
  const unsigned long long tag = ep * 0x9E3779B97F4A7C15ull + 0x632BE59BD9B4E019ull;
  STAMP(4);
  wbar<0>();
  STAMP(5);
  if (w == 0 && lane == 0) __hip_atomic_store(ctl + 16 * (kvh + 1), tag, __ATOMIC_RELAXED, __HIP_MEMORY_SCOPE_AGENT);
  if (w < 7) {
    for (unsigned spin = 0; spin < (1u << 22); ++spin) {
      unsigned long long f = tag;
      if (lane < 8) f = __hip_atomic_load(ctl + 16 * (lane + 1), __ATOMIC_RELAXED, __HIP_MEMORY_SCOPE_AGENT);
      if (__all(f == tag)) break;
      __builtin_amdgcn_s_sleep(2);
    }
    __builtin_amdgcn_fence(__ATOMIC_ACQUIRE, "wavefront");
    STAMP(6);
    const unsigned off = (unsigned)WS_PART + ((unsigned)(grp * 56 + kvh * 7 + w) << 13) + lane * 16;
    u32x4 v[8];
#pragma unroll
    for (int s = 0; s < 8; ++s) v[s] = __builtin_amdgcn_raw_buffer_load_b128(wsr, off + s * 1024, 0, 16);
    float acc[8];
#pragma unroll
    for (int e = 0; e < 8; ++e) acc[e] = 0.f;
#pragma unroll
    for (int s = 0; s < 8; ++s)
#pragma unroll
      for (int e = 0; e < 4; ++e) { acc[2 * e] += __uint_as_float(v[s][e] << 16); acc[2 * e + 1] += __uint_as_float(v[s][e] & 0xffff0000u); }
    const int q = 32 * (kvh >> 2) + li, n = 112 * (kvh & 3) + 16 * w + 4 * g;
    char* o = out + ((size_t)(b * SQ + q) * (2 * DM) + br * DM + n) * 2;
    *(u32x2*)o = u32x2{cvt2(acc[0], acc[1]), cvt2(acc[2], acc[3])};
    *(u32x2*)(o + 16 * 2 * DM * 2) = u32x2{cvt2(acc[4], acc[5]), cvt2(acc[6], acc[7])};
    STAMP(7);
    if (kvh == 0 && w == 0 && lane == 0) __hip_atomic_store(ctl, tag, __ATOMIC_RELAXED, __HIP_MEMORY_SCOPE_AGENT);
  }
}

extern "C" void kernel_launch(void* const* d_in, const int* in_sizes, int n_in, void* d_out, int out_size, void* d_ws, size_t ws_size,
                              hipStream_t stream) {
  (void)in_sizes; (void)n_in; (void)out_size; (void)ws_size;
  (void)hipFuncSetAttribute((const void*)gqa_fused_kernel, hipFuncAttributeMaxDynamicSharedMemorySize, LDS_BYTES);
  gqa_fused_kernel<<<256, 512, LDS_BYTES, stream>>>(
      (const char*)d_in[0], (const char*)d_in[1], (const char*)d_in[2], (const char*)d_in[3], (const char*)d_in[4],
      (const char*)d_in[5], (const char*)d_in[6], (const char*)d_in[7], (const char*)d_in[8], (const char*)d_in[9],
      (char*)d_out, (char*)d_ws);
}
```

```cpp
#include <hip/hip_runtime.h>
#include <hip/hip_bf16.h>
#include <stdint.h>
#include <type_traits>

typedef __attribute__((ext_vector_type(8))) short bf16x8;
typedef __attribute__((ext_vector_type(4))) short s16x4;
typedef __attribute__((ext_vector_type(4))) float f32x4;
typedef __attribute__((ext_vector_type(16))) float f32x16;
typedef __attribute__((ext_vector_type(2))) float f32x2;
typedef __attribute__((ext_vector_type(2))) __bf16 bf16x2_t;
typedef __attribute__((ext_vector_type(4))) unsigned u32x4;
typedef __attribute__((ext_vector_type(2))) unsigned u32x2;
#define LAS __attribute__((address_space(3)))
typedef LAS char* lds_ptr;

constexpr int SQ = 64, DM = 448, HKV = 8, SK = 1024, DH = 256;
constexpr int SS = 32768;
constexpr int MISC = 4 * SS;
constexpr int LDS_BYTES = 5 * SS;
constexpr int XIMG = 3 * SS;
constexpr int QST = 3 * SS;
constexpr int OST = 2 * SS;
constexpr float CEXP = 0.0625f * 1.4426950408889634f;
#define LMAX 1.0e18f
#define STAMP_ATT 0
#define LA 2
#define NBK 5
#define NBV 6
#define NBW 4
constexpr int RDEF = 2;

constexpr size_t WS_PART = 65536;

#define MFMA16(a, b, c) __builtin_amdgcn_mfma_f32_16x16x32_bf16((a), (b), (c), 0, 0, 0)
#define MFMA32(a, b, c) __builtin_amdgcn_mfma_f32_32x32x16_bf16((a), (b), (c), 0, 0, 0)
#define CBAR() asm volatile("s_waitcnt lgkmcnt(0)\n\ts_barrier" ::: "memory")
#define SBAR() __builtin_amdgcn_sched_barrier(0)
#define OPAQUE(x) asm volatile("" : "+v"(x))
#define PIN2(x, y) asm volatile("" : "+v"(x), "+v"(y))
#define SOPAQUE(x) asm volatile("" : "+s"(x))
#define DI __device__ __forceinline__
template <int N> DI void wbar() { asm volatile("s_waitcnt vmcnt(%0) lgkmcnt(0)\n\ts_barrier" ::"i"(N) : "memory"); }
DI void bar() { asm volatile("s_barrier" ::: "memory"); }
template <int N> using IC = std::integral_constant<int, N>;

DI unsigned cvt2(float a, float b) { f32x2 v = {a, b}; return __builtin_bit_cast(unsigned, __builtin_convertvector(v, bf16x2_t)); }
DI bf16x8 ldsb128(unsigned a) { return *(const LAS bf16x8*)(uintptr_t)a; }
DI s16x4 ldstr(unsigned a) { return __builtin_amdgcn_ds_read_tr16_b64_v4i16((LAS s16x4*)(uintptr_t)a); }
DI bf16x8 cat8(s16x4 a, s16x4 b) { return __builtin_shufflevector(a, b, 0, 1, 2, 3, 4, 5, 6, 7); }
DI int lane_id() { int l; asm volatile("v_mbcnt_lo_u32_b32 %0, -1, 0\n\tv_mbcnt_hi_u32_b32 %0, -1, %0" : "=v"(l)); return l; }
DI float xmax(float v) {
  { auto r = __builtin_amdgcn_permlane16_swap(__float_as_uint(v), __float_as_uint(v), false, false); v = fmaxf(__uint_as_float(r[0]), __uint_as_float(r[1])); }
  { auto r = __builtin_amdgcn_permlane32_swap(__float_as_uint(v), __float_as_uint(v), false, false); v = fmaxf(__uint_as_float(r[0]), __uint_as_float(r[1])); }
  return v;
}

DI void mm2(f32x4& c0, f32x4& c1, bf16x8 a, bf16x8 b0, bf16x8 b1) {
  asm volatile("v_mfma_f32_16x16x32_bf16 %0, %2, %3, %0\n\tv_mfma_f32_16x16x32_bf16 %1, %2, %4, %1" : "+v"(c0), "+v"(c1) : "v"(a), "v"(b0), "v"(b1));
}
DI void mm2z(f32x4& c0, f32x4& c1, bf16x8 a, bf16x8 b0, bf16x8 b1) {
  asm volatile("v_mfma_f32_16x16x32_bf16 %0, %2, %3, 0\n\tv_mfma_f32_16x16x32_bf16 %1, %2, %4, 0" : "=&v"(c0), "=&v"(c1) : "v"(a), "v"(b0), "v"(b1));
}
DI void mm2c(f32x4& c0, f32x4& c1, bf16x8 a, bf16x8 b0, bf16x8 b1, f32x4 i0, f32x4 i1) {
  asm volatile("v_mfma_f32_16x16x32_bf16 %0, %2, %3, %5\n\tv_mfma_f32_16x16x32_bf16 %1, %2, %4, %6" : "=&v"(c0), "=&v"(c1) : "v"(a), "v"(b0), "v"(b1), "v"(i0), "v"(i1));
}
#define MFMA_DRAIN() do { asm volatile("s_nop 7\n\ts_nop 1" ::: "memory"); SBAR(); } while (0)

DI void dma4(unsigned lds, const void* sbase, unsigned v0, unsigned v1, unsigned v2, unsigned v3) {
  unsigned keep;
  asm volatile("s_mov_b32 %0, m0\n\ts_mov_b32 m0, %1\n\ts_nop 3\n\t"
               "global_load_lds_dwordx4 %2, %6\n\tglobal_load_lds_dwordx4 %3, %6 offset:1024\n\t"
               "global_load_lds_dwordx4 %4, %6 offset:2048\n\tglobal_load_lds_dwordx4 %5, %6 offset:3072\n\ts_mov_b32 m0, %0"
               : "=&s"(keep) : "s"(lds), "v"(v0), "v"(v1), "v"(v2), "v"(v3), "s"(sbase) : "memory");
}
DI void dma2(unsigned lds, const void* sbase, unsigned v0, unsigned v1) {
  unsigned keep;
  asm volatile("s_mov_b32 %0, m0\n\ts_mov_b32 m0, %1\n\ts_nop 3\n\t"
               "global_load_lds_dwordx4 %2, %4\n\tglobal_load_lds_dwordx4 %3, %4 offset:1024\n\ts_mov_b32 m0, %0"
               : "=&s"(keep) : "s"(lds), "v"(v0), "v"(v1), "s"(sbase) : "memory");
}
DI void dma4s(unsigned lds, const char* s, size_t gstride, unsigned v) {
  unsigned keep;
  asm volatile("s_mov_b32 %0, m0\n\ts_mov_b32 m0, %1\n\ts_nop 3\n\tglobal_load_lds_dwordx4 %2, %3 nt\n\t"
               "s_add_u32 m0, m0, 0x2000\n\ts_nop 0\n\tglobal_load_lds_dwordx4 %2, %4 nt\n\t"
               "s_add_u32 m0, m0, 0x2000\n\ts_nop 0\n\tglobal_load_lds_dwordx4 %2, %5 nt\n\t"
               "s_add_u32 m0, m0, 0x2000\n\ts_nop 0\n\tglobal_load_lds_dwordx4 %2, %6 nt\n\ts_mov_b32 m0, %0"
               : "=&s"(keep) : "s"(lds), "v"(v), "s"(s), "s"(s + gstride), "s"(s + 2 * gstride), "s"(s + 3 * gstride) : "memory", "scc");
}

DI void dma4s_keep(unsigned lds, const char* s, size_t gstride, unsigned v) {
  unsigned keep;
  asm volatile("s_mov_b32 %0, m0\n\ts_mov_b32 m0, %1\n\ts_nop 3\n\tglobal_load_lds_dwordx4 %2, %3\n\t"
               "s_add_u32 m0, m0, 0x2000\n\ts_nop 0\n\tglobal_load_lds_dwordx4 %2, %4\n\t"
               "s_add_u32 m0, m0, 0x2000\n\ts_nop 0\n\tglobal_load_lds_dwordx4 %2, %5\n\t"
               "s_add_u32 m0, m0, 0x2000\n\ts_nop 0\n\tglobal_load_lds_dwordx4 %2, %6\n\ts_mov_b32 m0, %0"
               : "=&s"(keep) : "s"(lds), "v"(v), "s"(s), "s"(s + gstride), "s"(s + 2 * gstride), "s"(s + 3 * gstride) : "memory", "scc");
}

template <int NF, int NB, int NI, class RD, class MM, class PREV, class MID>
DI void ring_step(bf16x8 (&carry)[RDEF], RD rd, MM mm, PREV prev, MID mid) {
  static_assert(NI + RDEF <= NB, "");
  bf16x8 fb[NB];
#pragma unroll
  for (int f = 0; f < NI; ++f) fb[f] = rd(f);
  SBAR();
#pragma unroll
  for (int r = 0; r < RDEF; ++r) { prev(r, carry[r]); SBAR(); fb[NI + r] = rd(NI + r); SBAR(); }
  mid();
  SBAR();
#pragma unroll
  for (int f = NI + RDEF; f < NB; ++f) fb[f] = rd(f);
  SBAR();
#pragma unroll
  for (int f = 0; f < NF - RDEF; ++f) {
    mm(f, fb[f % NB]); SBAR();
    if (f + NB < NF) { fb[f % NB] = rd(f + NB); SBAR(); }
  }
#pragma unroll
  for (int r = 0; r < RDEF; ++r) carry[r] = fb[(NF - RDEF + r) % NB];
}

#define TS(k) do {} while (0)
#define STAMP(i) do {} while (0)

__global__ __launch_bounds__(512, 2) void gqa_fused_kernel(
    const char* __restrict__ xA, const char* __restrict__ kA, const char* __restrict__ vA, const char* __restrict__ wqA, const char* __restrict__ woA,
    const char* __restrict__ xB, const char* __restrict__ kB, const char* __restrict__ vB, const char* __restrict__ wqB, const char* __restrict__ woB,
    char* __restrict__ out, char* __restrict__ ws) {
  extern __shared__ __attribute__((aligned(16))) char smem[];
  const unsigned lds0 = (unsigned)(uintptr_t)(lds_ptr)smem;
  int w = __builtin_amdgcn_readfirstlane(threadIdx.x >> 6);
  const int id = blockIdx.x, br = (id >> 2) & 1, kvh = 2 * (id & 3) + ((id >> 3) & 1), b = id >> 4, grp = br * 16 + b;
  int rot7 = b % 7;
  int rg = w & 3, hf = w >> 2;
  STAMP(0);
  const char* kg = (br ? kB : kA) + (size_t)(b * HKV + kvh) * (SK * DH * 2);
  const char* vg = (br ? vB : vA) + (size_t)(b * HKV + kvh) * (SK * DH * 2);
  const char* wog = (br ? woB : woA) + (size_t)kvh * 512 * (DM * 2);

  auto k_eighth = [&](int t, int e) {
    const int ln = lane_id(), hi = ln >> 5;
    const char* kb = kg; SOPAQUE(kb);
    dma4s(lds0 + (t & 3) * SS + e * 1024, kb + (size_t)t * SS + e * 1024, 8192, hi * 512 + (((ln & 31) ^ (2 * e + hi)) << 4));
  };

  int vrot = (id >> 3) & 3;
  auto wo_eighth = [&](int T, unsigned base, int e) {
    const int ln = lane_id(), r8 = ln >> 3;
    int r = (T >> 1) + rot7; r = r >= 7 ? r - 7 : r;
    const char* wb = wog; SOPAQUE(wb);
    dma4s_keep(lds0 + base + e * 1024, wb + (size_t)((T & 1) * 256 + 8 * e) * 896 + r * 128, 64 * 896, r8 * 896 + (((ln & 7) ^ ((r8 & 2) << 1)) << 4));
  };
  auto issue = [&](int p) {
    if (p < 16) k_eighth(p, w);
    else if (p < 32) {
      const int ln = lane_id(), n = p - 16, r8 = ln >> 3;
      const char* vb = vg; SOPAQUE(vb);
      dma4s(lds0 + (p & 3) * SS + w * 1024, vb + (size_t)((n & 3) * 256 + 8 * w) * 512 + (((n >> 2) + vrot) & 3) * 128, 64 * 512, r8 * 512 + (((ln & 7) ^ (r8 & 6)) << 4));
    } else if (p < 34) wo_eighth(p - 32, (p & 3) * SS, w);
  };

  u32x4 pst[16][2];
  f32x4 oa[4][2];
  bf16x8 carry[RDEF];
  float mref[2] = {-INFINITY, -INFINITY};
  float cpk;

  auto vaddr = [&](int slot) {
    const int ln = lane_id(), rl = ((ln >> 4) << 2) | ((ln >> 2) & 3);
    unsigned a = lds0 + slot * SS + hf * 4096 + rl * 128 + ((((ln >> 1) & 1) ^ (rl & 6) ^ (hf << 2)) << 4) + (ln & 1) * 8;
    OPAQUE(a);
    return a;
  };
  auto vrd = [&](unsigned vb, int f) { const unsigned a = (vb ^ (unsigned)((f & 3) << 5)) + (f >> 2) * 8192; return cat8(ldstr(a), ldstr(a + 2048)); };
  auto vmm = [&](auto cc, int f, bf16x8 a) {
    constexpr int c = decltype(cc)::value;
    const bf16x8 b0 = __builtin_bit_cast(bf16x8, pst[4 * c + (f >> 2)][0]), b1 = __builtin_bit_cast(bf16x8, pst[4 * c + (f >> 2)][1]);
    if (c == 0 && f < 4) mm2z(oa[f & 3][0], oa[f & 3][1], a, b0, b1); else mm2(oa[f & 3][0], oa[f & 3][1], a, b0, b1);
  };
  auto nop = []() {};

  for (int attempt = 0;; ++attempt) {
  SOPAQUE(w); SOPAQUE(rot7); SOPAQUE(vrot); rg = w & 3; hf = w >> 2;
  if (w >= 4) {
    const int lw = w - 4, lane = lane_id();
    const char* xg = (br ? xB : xA) + (size_t)b * (SQ * DM * 2); SOPAQUE(xg);
    const char* wqg = (br ? wqB : wqA) + (size_t)kvh * 1024; SOPAQUE(wqg);
    unsigned wofs[4];
#pragma unroll
    for (int j = 0; j < 4; ++j) wofs[j] = j * (8192 - 1024) + ((lane ^ (j << 2)) << 4);
    int wsl = 1;
    auto wtile = [&](int i) {
      int G = (i >> 1) + rot7; G = G >= 7 ? G - 7 : G;
      const char* src = wqg + (size_t)(G * 64 + (i & 1) * 32 + 8 * lw) * 8192;
      const unsigned dst = lds0 + wsl * SS + lw * 8192;
      dma4(dst, src, wofs[0], wofs[1], wofs[2], wofs[3]);
      dma4(dst + 4096, src + 4 * 8192, wofs[0], wofs[1], wofs[2], wofs[3]);
      wsl = wsl == 2 ? 0 : wsl + 1;
    };
    const int xr = 16 * lw + (lane >> 3), xc = lane & 7;
    const unsigned x0 = xr * 896 + ((xc ^ ((xr >> 1) & 7)) << 4), x1 = (xr + 8) * 896 + ((xc ^ (((xr + 8) >> 1) & 7)) << 4) - 1024;
    auto xtile = [&](int Gi) { int G = Gi + rot7; G = G >= 7 ? G - 7 : G; dma2(lds0 + XIMG + Gi * 8192 + lw * 2048, xg + G * 128, x0, x1); };
    xtile(0); wtile(0); wtile(1);
    for (int i = 0; i < 14; ++i) {
      if (i == 1) wbar<20>(); else wbar<8>();
      if (i == 0) for (int Gi = 1; Gi < 7; ++Gi) xtile(Gi);
      if (i + 2 < 14) wtile(i + 2); else { k_eighth(i - 12, 2 * lw); k_eighth(i - 12, 2 * lw + 1); }
    }
    bar();
  } else {
    const int lane = lane_id(), g = lane >> 4, qq = (lane >> 2) & 3, pp = lane & 3;
    const int mh = w & 1, hh = w >> 1, h = lane >> 5, m = lane & 31;
    f32x16 qa[8];
#pragma unroll
    for (int a = 0; a < 8; ++a) qa[a] = (f32x16)(0.f);
    const unsigned wbase = lds0 + (8 * h + qq) * 1024 + hh * 512 + (((2 * (g & 1) + (pp >> 1)) ^ (qq << 2)) << 4) + (pp & 1) * 8;
    const unsigned xbase = lds0 + XIMG + (32 * mh + m) * 128 + ((h ^ ((m >> 1) & 7)) << 4);
    bf16x8 wfA[8], wfB[8], xa, xb, xo;
#pragma unroll
    for (int a = 0; a < 8; ++a) wfB[a] = (bf16x8)(0);
    xo = (bf16x8)(0);
    int sl = SS;
    for (int i = 0; i < 14; ++i) {
      CBAR();
      unsigned p = wbase + sl; OPAQUE(p);
      sl = sl == 2 * SS ? 0 : sl + SS;
      const unsigned xp = (xbase + (i >> 1) * 8192) ^ (unsigned)((i & 1) << 6);
      xa = ldsb128(xp); xb = ldsb128(xp ^ 32);
      SBAR();
#pragma unroll
      for (int a = 0; a < 8; ++a) {
        const unsigned q = (p ^ (unsigned)((a & 3) << 6)) + (a >> 2) * 256; wfA[a] = cat8(ldstr(q), ldstr(q + 4096));
        qa[a] = MFMA32(wfB[a], xo, qa[a]);
        SBAR();
      }
#pragma unroll
      for (int a = 0; a < 8; ++a) {
        if (a < 4) {
#pragma unroll
          for (int u = 0; u < 2; ++u) { const int a2 = 2 * a + u; const unsigned q = (p ^ (unsigned)((a2 & 3) << 6)) + (a2 >> 2) * 256; wfB[a2] = cat8(ldstr(q + 16384), ldstr(q + 16384 + 4096)); }
        }
        qa[a] = MFMA32(wfA[a], xa, qa[a]);
        SBAR();
      }
      xo = xb;
    }
    CBAR();
    const unsigned qw = lds0 + QST + (hh * 2 + mh) * 16384 + (m >> 4) * 8192 + (m & 15) * 16 + h * 8;
#pragma unroll
    for (int nb = 0; nb < 8; ++nb) {
      qa[nb] = MFMA32(wfB[nb], xo, qa[nb]);
#pragma unroll
      for (int a = 0; a < 4; ++a)
        *(LAS u32x2*)(uintptr_t)(qw + nb * 1024 + a * 256) =
            u32x2{cvt2(qa[nb][4 * a] * CEXP, qa[nb][4 * a + 1] * CEXP), cvt2(qa[nb][4 * a + 2] * CEXP, qa[nb][4 * a + 3] * CEXP)};
    }
  }
  CBAR();
  bf16x8 qf[2][8];
  {
    const unsigned qr = lds0 + QST + rg * 16384 + lane_id() * 16;
#pragma unroll
    for (int qb = 0; qb < 2; ++qb)
#pragma unroll
      for (int s = 0; s < 8; ++s) qf[qb][s] = ldsb128(qr + (qb * 8 + s) * 1024);
  }
  if (attempt == STAMP_ATT) STAMP(1);

    f32x4 sa[2][2];
    f32x4 nm[2];
    float tmax[2] = {0.f, 0.f};
    auto kmm = [&](auto tt, int f, bf16x8 a) {
      constexpr int t = decltype(tt)::value;
      if (f >= 2) mm2(sa[f & 1][0], sa[f & 1][1], a, qf[0][f >> 1], qf[1][f >> 1]);
      else if (t == 0) mm2z(sa[f][0], sa[f][1], a, qf[0][0], qf[1][0]);
      else mm2c(sa[f][0], sa[f][1], a, qf[0][0], qf[1][0], nm[0], nm[1]);
    };
    auto smax = [&](int qb) {
      return fmaxf(fmaxf(fmaxf(sa[0][qb][0], sa[0][qb][1]), fmaxf(sa[0][qb][2], sa[0][qb][3])), fmaxf(fmaxf(sa[1][qb][0], sa[1][qb][1]), fmaxf(sa[1][qb][2], sa[1][qb][3])));
    };
    auto soft = [&](auto tt) {
      constexpr int t = decltype(tt)::value;
      MFMA_DRAIN();
#pragma unroll
      for (int qb = 0; qb < 2; ++qb) {
        if (t == 0) {
          mref[qb] = fmaxf(mref[qb], xmax(smax(qb)));
          nm[qb] = (f32x4)(-mref[qb]); OPAQUE(nm[qb]);
          sa[0][qb] += nm[qb]; sa[1][qb] += nm[qb];
        } else if (attempt == 1) tmax[qb] = fmaxf(tmax[qb], smax(qb));
        float e[8];
#pragma unroll
        for (int r = 0; r < 8; ++r) e[r] = __builtin_amdgcn_exp2f(sa[r >> 2][qb][r & 3]);
        pst[t][qb] = u32x4{cvt2(e[0], e[1]), cvt2(e[2], e[3]), cvt2(e[4], e[5]), cvt2(e[6], e[7])};
        OPAQUE(pst[t][qb]);
      }
    };
    auto kstep = [&](auto tt) {
      constexpr int t = decltype(tt)::value;
      const int ln = lane_id();
      unsigned kb0 = lds0 + (t & 3) * SS + hf * 16384 + (ln & 15) * 512 + (((ln >> 4) ^ (ln & 15)) << 4); OPAQUE(kb0);
      if (t == 0) wbar<8>(); else wbar<4 * (LA - 1)>();
      if (LA == 3 && t == 0) issue(2);
      if (hf == 1) issue(t + LA);
      ring_step<16, NBK, 2>(carry,
          [&](int f) { return ldsb128((kb0 ^ (unsigned)(((f >> 1) & 3) << 6)) + (f >> 3) * 256 + (f & 1) * 8192); },
          [&](int f, bf16x8 a) { kmm(IC<t>{}, f, a); },
          [&](int r, bf16x8 a) { if constexpr (t > 0) kmm(IC<t - 1>{}, 16 - RDEF + r, a); },
          [&]() { if constexpr (t > 0) soft(IC<t - 1>{}); });
      if (hf == 0) issue(t + LA);
    };
    kstep(IC<0>{}); kstep(IC<1>{}); kstep(IC<2>{}); kstep(IC<3>{}); kstep(IC<4>{}); kstep(IC<5>{}); kstep(IC<6>{}); kstep(IC<7>{});
    kstep(IC<8>{}); kstep(IC<9>{}); kstep(IC<10>{}); kstep(IC<11>{}); kstep(IC<12>{}); kstep(IC<13>{}); kstep(IC<14>{}); kstep(IC<15>{});
    if (attempt == STAMP_ATT) STAMP(2);
    f32x4 la[2];
    bf16x8 ones;
#pragma unroll
    for (int j = 0; j < 8; ++j) ones[j] = 0x3f80;
    auto lmm = [&](auto cc) {
      constexpr int c = decltype(cc)::value;
#pragma unroll
      for (int u = 0; u < 4; ++u)
#pragma unroll
        for (int qb = 0; qb < 2; ++qb) la[qb] = MFMA16(ones, __builtin_bit_cast(bf16x8, pst[4 * c + u][qb]), (c == 0 && u == 0) ? (f32x4)(0.f) : la[qb]);
    };
    {
      const unsigned vb = vaddr(0); wbar<4 * (LA - 1)>(); if (hf == 1) issue(16 + LA);
      ring_step<16, NBV, 2>(carry, [&](int f) { return vrd(vb, f); }, [&](int f, bf16x8 a) { vmm(IC<0>{}, f, a); },
                         [&](int r, bf16x8 a) { kmm(IC<15>{}, 16 - RDEF + r, a); }, [&]() { soft(IC<15>{}); lmm(IC<0>{}); });
      if (hf == 0) issue(16 + LA);
    }
    {
      const unsigned vb = vaddr(1); wbar<4 * (LA - 1)>(); if (hf == 1) issue(17 + LA);
      ring_step<16, NBV, 2>(carry, [&](int f) { return vrd(vb, f); }, [&](int f, bf16x8 a) { vmm(IC<1>{}, f, a); },
                         [&](int r, bf16x8 a) { vmm(IC<0>{}, 16 - RDEF + r, a); }, [&]() { lmm(IC<1>{}); });
      if (hf == 0) issue(17 + LA);
    }
    {
      const unsigned vb = vaddr(2); wbar<4 * (LA - 1)>(); if (hf == 1) issue(18 + LA);
      ring_step<16, NBV, 2>(carry, [&](int f) { return vrd(vb, f); }, [&](int f, bf16x8 a) { vmm(IC<2>{}, f, a); },
                         [&](int r, bf16x8 a) { vmm(IC<1>{}, 16 - RDEF + r, a); }, [&]() { lmm(IC<2>{}); lmm(IC<3>{}); });
      const int ln = lane_id();
      const bool bad = !(la[0][0] <= LMAX) || !(la[1][0] <= LMAX);
      const float anybad = __any(bad) ? 1.f : 0.f;
      if (ln < 16) *(LAS f32x4*)(uintptr_t)(lds0 + MISC + w * 256 + ln * 16) = f32x4{mref[0], la[0][0], mref[1], la[1][0]};
      if (ln == 0) *(LAS float*)(uintptr_t)(lds0 + MISC + 2048 + w * 4) = anybad;
      if (hf == 0) issue(18 + LA);
    }
    float fl;
    {
      const unsigned vb = vaddr(3); wbar<4 * (LA - 1)>(); if (hf == 1) issue(19 + LA);
      ring_step<16, NBV, 2>(carry, [&](int f) { return vrd(vb, f); }, [&](int f, bf16x8 a) { vmm(IC<3>{}, f, a); },
                         [&](int r, bf16x8 a) { vmm(IC<2>{}, 16 - RDEF + r, a); },
                         [&]() {
                           const int ln = lane_id();
                           const f32x4 o = *(const LAS f32x4*)(uintptr_t)(lds0 + MISC + (w ^ 4) * 256 + (ln & 15) * 16);
                           const f32x4 f0 = *(const LAS f32x4*)(uintptr_t)(lds0 + MISC + 2048), f1 = *(const LAS f32x4*)(uintptr_t)(lds0 + MISC + 2048 + 16);
                           fl = (f0[0] + f0[1]) + (f0[2] + f0[3]) + (f1[0] + f1[1]) + (f1[2] + f1[3]); OPAQUE(fl);
                           const bool odd = (ln >> 4) & 1;
                           float l0 = la[0][0], l1 = la[1][0], m0 = mref[0], m1 = mref[1], o0_ = o[0], o1_ = o[1], o2_ = o[2], o3_ = o[3];
                           OPAQUE(l0); OPAQUE(l1); OPAQUE(m0); OPAQUE(m1); OPAQUE(o0_); OPAQUE(o1_); OPAQUE(o2_); OPAQUE(o3_);
                           const float ms = odd ? m1 : m0, ls = odd ? l1 : l0, mo = odd ? o2_ : o0_, lo = odd ? o3_ : o1_;
                           const float m = fmaxf(ms, mo), ea = __builtin_amdgcn_exp2f(ms - m), eb = __builtin_amdgcn_exp2f(mo - m);
                           cpk = ea / (ls * ea + lo * eb); OPAQUE(cpk);
                         });
      if (hf == 0) issue(19 + LA);
    }
    if (attempt == 2 || (attempt == 0 && __builtin_amdgcn_readfirstlane(__float_as_uint(fl)) == 0u)) break;
    mref[0] += xmax(tmax[0]); mref[1] += xmax(tmax[1]);
    wbar<0>();
  }

  u32x4 ost[4][2];
  auto comb_write = [&]() {
    MFMA_DRAIN();
    const int ln = lane_id();
    float cc = cpk; OPAQUE(cc);
    const auto cr = __builtin_amdgcn_permlane16_swap(__float_as_uint(cc), __float_as_uint(cc), false, false);
    const float c0 = __uint_as_float(cr[0]), c1 = __uint_as_float(cr[1]);
#pragma unroll
    for (int db = 0; db < 4; ++db) { oa[db][0] *= c0; oa[db][1] *= c1; }
#pragma unroll
    for (int k = 0; k < 2; ++k)
#pragma unroll
      for (int qb = 0; qb < 2; ++qb) *(LAS f32x4*)(uintptr_t)(lds0 + MISC + w * 4096 + (k * 2 + qb) * 1024 + ln * 16) = oa[2 + k][qb];
  };
  f32x4 okeep[2][2];
  auto comb_read = [&](auto jj) {
    constexpr int j = decltype(jj)::value;
    const unsigned pad = lds0 + MISC + (w ^ 4) * 4096 + lane_id() * 16;
#pragma unroll
    for (int qb = 0; qb < 2; ++qb) {
      const f32x4 a = *(const LAS f32x4*)(uintptr_t)(pad + qb * 1024) + okeep[0][qb];
      const f32x4 c = *(const LAS f32x4*)(uintptr_t)(pad + (2 + qb) * 1024) + okeep[1][qb];
      ost[j][qb] = u32x4{cvt2(a[0], a[1]), cvt2(a[2], a[3]), cvt2(c[0], c[1]), cvt2(c[2], c[3])};
      OPAQUE(ost[j][qb]);
    }
  };
  auto vstep = [&](auto nn) {
    constexpr int n = decltype(nn)::value, c = n & 3;
    const unsigned vb = vaddr(n & 3);
    wbar<4 * (LA - 1)>();
    if (hf == 1) issue(n + 16 + LA);
    ring_step<16, NBV, 2>(carry, [&](int f) { return vrd(vb, f); }, [&](int f, bf16x8 a) { vmm(IC<c>{}, f, a); },
                          [&](int r, bf16x8 a) { vmm(IC<(c + 3) & 3>{}, 16 - RDEF + r, a); },
                          [&]() {
                            if constexpr (c == 0) { comb_write(); okeep[0][0] = oa[0][0]; okeep[0][1] = oa[0][1]; okeep[1][0] = oa[1][0]; okeep[1][1] = oa[1][1];
                                                    PIN2(okeep[0][0], okeep[0][1]); PIN2(okeep[1][0], okeep[1][1]); }
                            if constexpr (c == 1) comb_read(IC<(n >> 2) - 1>{});
                          });
    if (hf == 0) issue(n + 16 + LA);
  };
  vstep(IC<4>{}); vstep(IC<5>{}); vstep(IC<6>{}); vstep(IC<7>{}); vstep(IC<8>{}); vstep(IC<9>{});
  vstep(IC<10>{}); vstep(IC<11>{}); vstep(IC<12>{}); vstep(IC<13>{}); vstep(IC<14>{}); vstep(IC<15>{});
  STAMP(3);
#pragma unroll
  for (int r = 0; r < RDEF; ++r) vmm(IC<3>{}, 16 - RDEF + r, carry[r]);
  comb_write(); okeep[0][0] = oa[0][0]; okeep[0][1] = oa[0][1]; okeep[1][0] = oa[1][0]; okeep[1][1] = oa[1][1];
  CBAR();
  comb_read(IC<3>{});
  {
    const int ln = lane_id(), g = ln >> 4;
    const unsigned ow = lds0 + OST + ((rg & 1) * 2 + (rg >> 1)) * 16384 + hf * 2048 + ((g >> 1) * 32 + (ln & 15)) * 16 + (g & 1) * 8;
#pragma unroll
    for (int j = 0; j < 4; ++j) {
      const unsigned oj = ow + ((j + vrot) & 3) * 4096;
#pragma unroll
      for (int qb = 0; qb < 2; ++qb) {
        *(LAS u32x2*)(uintptr_t)(oj + qb * 256) = u32x2{ost[j][qb][0], ost[j][qb][1]};
        *(LAS u32x2*)(uintptr_t)(oj + qb * 256 + 1024) = u32x2{ost[j][qb][2], ost[j][qb][3]};
      }
    }
  }
  wbar<4>();
  unsigned long long* ctl = (unsigned long long*)(ws + (size_t)grp * 2048);
  const __amdgpu_buffer_rsrc_t wsr = __builtin_amdgcn_make_buffer_rsrc(ws, 0, 0x7fffffff, 0x00020000);
  unsigned long long ep;
  if (w >= 4) {
    const int lw = w - 4;
    wo_eighth(2, MISC, 2 * lw); wo_eighth(2, MISC, 2 * lw + 1);
    int base = 0;
    for (int T = 1; T < 14; ++T) {
      if (T < 13) wbar<8>(); else wbar<0>();
      if (T + 2 < 14) { wo_eighth(T + 2, base, 2 * lw); wo_eighth(T + 2, base, 2 * lw + 1); }
      base = base == 0 ? SS : (base == SS ? MISC : 0);
    }
    ep = __hip_atomic_load(ctl, __ATOMIC_RELAXED, __HIP_MEMORY_SCOPE_AGENT);
  } else {
    const int ln = lane_id(), g = ln >> 4, qq = (ln >> 2) & 3, pp = ln & 3, h = ln >> 5;
    const int qh = w & 1, nh = w >> 1;
    bf16x8 of[2][16];
#pragma unroll
    for (int hh = 0; hh < 2; ++hh)
#pragma unroll
      for (int s = 0; s < 16; ++s) of[hh][s] = ldsb128(lds0 + OST + (qh * 2 + hh) * 16384 + s * 1024 + ln * 16);
    ep = __hip_atomic_load(ctl, __ATOMIC_RELAXED, __HIP_MEMORY_SCOPE_AGENT);
    const unsigned abase = lds0 + (8 * h + qq) * 128 + (((4 * nh + 2 * (g & 1) + (pp >> 1)) ^ ((qq & 2) << 1)) << 4) + (pp & 1) * 8;
    bf16x8 fa[8], fbb[8];
#pragma unroll
    for (int a = 0; a < 8; ++a) fbb[a] = (bf16x8)(0);
    f32x16 acc = (f32x16)(0.f), acc2 = (f32x16)(0.f);
    int base = 0;
    auto ostore = [&](int rr) {
      int r = rr + rot7; r = r >= 7 ? r - 7 : r;
      acc += acc2; acc2 = (f32x16)(0.f);
#pragma unroll
      for (int a = 0; a < 2; ++a) {
        u32x4 f = {cvt2(acc[8 * a], acc[8 * a + 1]), cvt2(acc[8 * a + 2], acc[8 * a + 3]), cvt2(acc[8 * a + 4], acc[8 * a + 5]), cvt2(acc[8 * a + 6], acc[8 * a + 7])};
        *(LAS u32x4*)(uintptr_t)(lds0 + OST + (r * 8 + w * 2 + a) * 1024 + ln * 16) = f;
      }
      acc = (f32x16)(0.f);
    };
    auto otile = [&](auto hh_, bool first, int rr) {
      constexpr int hh = decltype(hh_)::value;
      unsigned p = abase + base; OPAQUE(p);
      base = base == 0 ? SS : (base == SS ? MISC : 0);
#pragma unroll
      for (int a = 0; a < 8; ++a) {
        fa[a] = cat8(ldstr(p + a * 2048), ldstr(p + a * 2048 + 512));
        if (a & 1) acc2 = MFMA32(fbb[a], of[hh ^ 1][8 + a], acc2); else acc = MFMA32(fbb[a], of[hh ^ 1][8 + a], acc);
        SBAR();
      }
      if (hh == 0 && !first) ostore(rr - 1);
      SBAR();
#pragma unroll
      for (int a = 0; a < 8; ++a) {
        if (a < 4) {
#pragma unroll
          for (int u = 0; u < 2; ++u) fbb[2 * a + u] = cat8(ldstr(p + (8 + 2 * a + u) * 2048), ldstr(p + (8 + 2 * a + u) * 2048 + 512));
        }
        if (a & 1) acc2 = MFMA32(fa[a], of[hh][a], acc2); else acc = MFMA32(fa[a], of[hh][a], acc);
        SBAR();
      }
    };
    for (int rr = 0; rr < 7; ++rr) {
      if (rr > 0) CBAR();
      otile(IC<0>{}, rr == 0, rr);
      if (rr == 0) wbar<1>(); else CBAR();
      otile(IC<1>{}, false, rr);
    }
#pragma unroll
    for (int a = 0; a < 8; ++a) { if (a & 1) acc2 = MFMA32(fbb[a], of[1][8 + a], acc2); else acc = MFMA32(fbb[a], of[1][8 + a], acc); }
    ostore(6);
  }
  const int lane = lane_id();
  CBAR();
#pragma unroll
  for (int i = 0; i < 7; ++i) {
    const u32x4 f = *(const LAS u32x4*)(uintptr_t)(lds0 + OST + (i * 8 + w) * 1024 + lane * 16);
    const unsigned off = (unsigned)WS_PART + (((unsigned)(grp * 56 + i * 8 + w) * 8 + kvh) << 10) + lane * 16;
    __builtin_amdgcn_raw_buffer_store_b128(f, wsr, off, 0, 16);
  }
  const unsigned long long tag = ep * 0x9E3779B97F4A7C15ull + 0x632BE59BD9B4E019ull;
  STAMP(4);
  wbar<0>();
  STAMP(5);
  if (w == 0 && lane == 0) __hip_atomic_store(ctl + 16 * (kvh + 1), tag, __ATOMIC_RELAXED, __HIP_MEMORY_SCOPE_AGENT);
  if (w < 7) {
    for (unsigned spin = 0; spin < (1u << 22); ++spin) {
      unsigned long long f = tag;
      if (lane < 8) f = __hip_atomic_load(ctl + 16 * (lane + 1), __ATOMIC_RELAXED, __HIP_MEMORY_SCOPE_AGENT);
      if (__all(f == tag)) break;
      __builtin_amdgcn_s_sleep(2);
    }
    __builtin_amdgcn_fence(__ATOMIC_ACQUIRE, "wavefront");
    STAMP(6);
    const int pc = kvh * 7 + w, r = pc >> 3, pw = (pc >> 1) & 3, a = pc & 1;
    const unsigned off = (unsigned)WS_PART + ((unsigned)(grp * 56 + pc) << 13) + lane * 16;
    u32x4 v[8];
#pragma unroll
    for (int s = 0; s < 8; ++s) v[s] = __builtin_amdgcn_raw_buffer_load_b128(wsr, off + s * 1024, 0, 16);
    float acc[8];
#pragma unroll
    for (int e = 0; e < 8; ++e) acc[e] = 0.f;
#pragma unroll
    for (int s = 0; s < 8; ++s)
#pragma unroll
      for (int e = 0; e < 4; ++e) { acc[2 * e] += __uint_as_float(v[s][e] << 16); acc[2 * e + 1] += __uint_as_float(v[s][e] & 0xffff0000u); }
    const int q = 32 * (pw & 1) + (lane & 31), n = 64 * r + 32 * (pw >> 1) + 16 * a + 4 * (lane >> 5);
    char* o = out + ((size_t)(b * SQ + q) * (2 * DM) + br * DM + n) * 2;
    *(u32x2*)o = u32x2{cvt2(acc[0], acc[1]), cvt2(acc[2], acc[3])};
    *(u32x2*)(o + 16) = u32x2{cvt2(acc[4], acc[5]), cvt2(acc[6], acc[7])};
    STAMP(7);
    if (kvh == 0 && w == 0 && lane == 0) __hip_atomic_store(ctl, tag, __ATOMIC_RELAXED, __HIP_MEMORY_SCOPE_AGENT);
  }
}

extern "C" void kernel_launch(void* const* d_in, const int* in_sizes, int n_in, void* d_out, int out_size, void* d_ws, size_t ws_size,
                              hipStream_t stream) {
  (void)in_sizes; (void)n_in; (void)out_size; (void)ws_size;
  (void)hipFuncSetAttribute((const void*)gqa_fused_kernel, hipFuncAttributeMaxDynamicSharedMemorySize, LDS_BYTES);
  gqa_fused_kernel<<<256, 512, LDS_BYTES, stream>>>(
      (const char*)d_in[0], (const char*)d_in[1], (const char*)d_in[2], (const char*)d_in[3], (const char*)d_in[4],
      (const char*)d_in[5], (const char*)d_in[6], (const char*)d_in[7], (const char*)d_in[8], (const char*)d_in[9],
      (char*)d_out, (char*)d_ws);
}
```

```cpp
#include <hip/hip_runtime.h>
#include <hip/hip_bf16.h>
#include <stdint.h>
#include <type_traits>

typedef __attribute__((ext_vector_type(8))) short bf16x8;
typedef __attribute__((ext_vector_type(4))) short s16x4;
typedef __attribute__((ext_vector_type(4))) float f32x4;
typedef __attribute__((ext_vector_type(16))) float f32x16;
typedef __attribute__((ext_vector_type(2))) float f32x2;
typedef __attribute__((ext_vector_type(2))) __bf16 bf16x2_t;
typedef __attribute__((ext_vector_type(4))) unsigned u32x4;
typedef __attribute__((ext_vector_type(2))) unsigned u32x2;
#define LAS __attribute__((address_space(3)))
typedef LAS char* lds_ptr;

constexpr int SQ = 64, DM = 448, HKV = 8, SK = 1024, DH = 256;
constexpr int SS = 32768;
constexpr int MISC = 4 * SS;
constexpr int LDS_BYTES = 5 * SS;
constexpr int XIMG = 3 * SS;
constexpr int QST = 3 * SS;
constexpr float CEXP = 0.0625f * 1.4426950408889634f;
#define LMAX 1.0e18f
#define STAMP_ATT 0
#define NBK 5
#define NBV 6
#define NBW 4
constexpr int RDEF = 2;

constexpr size_t WS_PART = 65536;

#define MFMA16(a, b, c) __builtin_amdgcn_mfma_f32_16x16x32_bf16((a), (b), (c), 0, 0, 0)
#define MFMA32(a, b, c) __builtin_amdgcn_mfma_f32_32x32x16_bf16((a), (b), (c), 0, 0, 0)
#define CBAR() asm volatile("s_waitcnt lgkmcnt(0)\n\ts_barrier" ::: "memory")
#define SBAR() __builtin_amdgcn_sched_barrier(0)
#define OPAQUE(x) asm volatile("" : "+v"(x))
#define PIN2(x, y) asm volatile("" : "+v"(x), "+v"(y))
#define SOPAQUE(x) asm volatile("" : "+s"(x))
#define DI __device__ __forceinline__
template <int N> DI void wbar() { asm volatile("s_waitcnt vmcnt(%0) lgkmcnt(0)\n\ts_barrier" ::"i"(N) : "memory"); }
DI void bar() { asm volatile("s_barrier" ::: "memory"); }
template <int N> using IC = std::integral_constant<int, N>;

DI unsigned cvt2(float a, float b) { f32x2 v = {a, b}; return __builtin_bit_cast(unsigned, __builtin_convertvector(v, bf16x2_t)); }
DI bf16x8 ldsb128(unsigned a) { return *(const LAS bf16x8*)(uintptr_t)a; }
DI s16x4 ldstr(unsigned a) { return __builtin_amdgcn_ds_read_tr16_b64_v4i16((LAS s16x4*)(uintptr_t)a); }
DI bf16x8 cat8(s16x4 a, s16x4 b) { return __builtin_shufflevector(a, b, 0, 1, 2, 3, 4, 5, 6, 7); }
DI int lane_id() { int l; asm volatile("v_mbcnt_lo_u32_b32 %0, -1, 0\n\tv_mbcnt_hi_u32_b32 %0, -1, %0" : "=v"(l)); return l; }
DI float xmax(float v) {
  { auto r = __builtin_amdgcn_permlane16_swap(__float_as_uint(v), __float_as_uint(v), false, false); v = fmaxf(__uint_as_float(r[0]), __uint_as_float(r[1])); }
  { auto r = __builtin_amdgcn_permlane32_swap(__float_as_uint(v), __float_as_uint(v), false, false); v = fmaxf(__uint_as_float(r[0]), __uint_as_float(r[1])); }
  return v;
}

DI void mm2(f32x4& c0, f32x4& c1, bf16x8 a, bf16x8 b0, bf16x8 b1) {
  asm volatile("v_mfma_f32_16x16x32_bf16 %0, %2, %3, %0\n\tv_mfma_f32_16x16x32_bf16 %1, %2, %4, %1" : "+v"(c0), "+v"(c1) : "v"(a), "v"(b0), "v"(b1));
}
DI void mm2z(f32x4& c0, f32x4& c1, bf16x8 a, bf16x8 b0, bf16x8 b1) {
  asm volatile("v_mfma_f32_16x16x32_bf16 %0, %2, %3, 0\n\tv_mfma_f32_16x16x32_bf16 %1, %2, %4, 0" : "=&v"(c0), "=&v"(c1) : "v"(a), "v"(b0), "v"(b1));
}
DI void mm2c(f32x4& c0, f32x4& c1, bf16x8 a, bf16x8 b0, bf16x8 b1, f32x4 i0, f32x4 i1) {
  asm volatile("v_mfma_f32_16x16x32_bf16 %0, %2, %3, %5\n\tv_mfma_f32_16x16x32_bf16 %1, %2, %4, %6" : "=&v"(c0), "=&v"(c1) : "v"(a), "v"(b0), "v"(b1), "v"(i0), "v"(i1));
}
#define MFMA_DRAIN() do { asm volatile("s_nop 7\n\ts_nop 1" ::: "memory"); SBAR(); } while (0)

DI void dma4(unsigned lds, const void* sbase, unsigned v0, unsigned v1, unsigned v2, unsigned v3) {
  unsigned keep;
  asm volatile("s_mov_b32 %0, m0\n\ts_mov_b32 m0, %1\n\ts_nop 3\n\t"
               "global_load_lds_dwordx4 %2, %6\n\tglobal_load_lds_dwordx4 %3, %6 offset:1024\n\t"
               "global_load_lds_dwordx4 %4, %6 offset:2048\n\tglobal_load_lds_dwordx4 %5, %6 offset:3072\n\ts_mov_b32 m0, %0"
               : "=&s"(keep) : "s"(lds), "v"(v0), "v"(v1), "v"(v2), "v"(v3), "s"(sbase) : "memory");
}
DI void dma2(unsigned lds, const void* sbase, unsigned v0, unsigned v1) {
  unsigned keep;
  asm volatile("s_mov_b32 %0, m0\n\ts_mov_b32 m0, %1\n\ts_nop 3\n\t"
               "global_load_lds_dwordx4 %2, %4\n\tglobal_load_lds_dwordx4 %3, %4 offset:1024\n\ts_mov_b32 m0, %0"
               : "=&s"(keep) : "s"(lds), "v"(v0), "v"(v1), "s"(sbase) : "memory");
}
DI void dma4s(unsigned lds, const char* s, size_t gstride, unsigned v) {
  unsigned keep;
  asm volatile("s_mov_b32 %0, m0\n\ts_mov_b32 m0, %1\n\ts_nop 3\n\tglobal_load_lds_dwordx4 %2, %3 nt\n\t"
               "s_add_u32 m0, m0, 0x2000\n\ts_nop 0\n\tglobal_load_lds_dwordx4 %2, %4 nt\n\t"
               "s_add_u32 m0, m0, 0x2000\n\ts_nop 0\n\tglobal_load_lds_dwordx4 %2, %5 nt\n\t"
               "s_add_u32 m0, m0, 0x2000\n\ts_nop 0\n\tglobal_load_lds_dwordx4 %2, %6 nt\n\ts_mov_b32 m0, %0"
               : "=&s"(keep) : "s"(lds), "v"(v), "s"(s), "s"(s + gstride), "s"(s + 2 * gstride), "s"(s + 3 * gstride) : "memory", "scc");
}

DI void dma4s_keep(unsigned lds, const char* s, size_t gstride, unsigned v) {
  unsigned keep;
  asm volatile("s_mov_b32 %0, m0\n\ts_mov_b32 m0, %1\n\ts_nop 3\n\tglobal_load_lds_dwordx4 %2, %3\n\t"
               "s_add_u32 m0, m0, 0x2000\n\ts_nop 0\n\tglobal_load_lds_dwordx4 %2, %4\n\t"
               "s_add_u32 m0, m0, 0x2000\n\ts_nop 0\n\tglobal_load_lds_dwordx4 %2, %5\n\t"
               "s_add_u32 m0, m0, 0x2000\n\ts_nop 0\n\tglobal_load_lds_dwordx4 %2, %6\n\ts_mov_b32 m0, %0"
               : "=&s"(keep) : "s"(lds), "v"(v), "s"(s), "s"(s + gstride), "s"(s + 2 * gstride), "s"(s + 3 * gstride) : "memory", "scc");
}

template <int NF, int NB, int NI, class RD, class MM, class PREV, class MID>
DI void ring_step(bf16x8 (&carry)[RDEF], RD rd, MM mm, PREV prev, MID mid) {
  static_assert(NI + RDEF <= NB, "");
  bf16x8 fb[NB];
#pragma unroll
  for (int f = 0; f < NI; ++f) fb[f] = rd(f);
  SBAR();
#pragma unroll
  for (int r = 0; r < RDEF; ++r) { prev(r, carry[r]); SBAR(); fb[NI + r] = rd(NI + r); SBAR(); }
  mid();
  SBAR();
#pragma unroll
  for (int f = NI + RDEF; f < NB; ++f) fb[f] = rd(f);
  SBAR();
#pragma unroll
  for (int f = 0; f < NF - RDEF; ++f) {
    mm(f, fb[f % NB]); SBAR();
    if (f + NB < NF) { fb[f % NB] = rd(f + NB); SBAR(); }
  }
#pragma unroll
  for (int r = 0; r < RDEF; ++r) carry[r] = fb[(NF - RDEF + r) % NB];
}

#define TS(k) do {} while (0)
#define STAMP(i) do {} while (0)

__global__ __launch_bounds__(512, 2) void gqa_fused_kernel(
    const char* __restrict__ xA, const char* __restrict__ kA, const char* __restrict__ vA, const char* __restrict__ wqA, const char* __restrict__ woA,
    const char* __restrict__ xB, const char* __restrict__ kB, const char* __restrict__ vB, const char* __restrict__ wqB, const char* __restrict__ woB,
    char* __restrict__ out, char* __restrict__ ws) {
  extern __shared__ __attribute__((aligned(16))) char smem[];
  const unsigned lds0 = (unsigned)(uintptr_t)(lds_ptr)smem;
  int w = __builtin_amdgcn_readfirstlane(threadIdx.x >> 6);
  const int id = blockIdx.x, br = (id >> 2) & 1, kvh = 2 * (id & 3) + ((id >> 3) & 1), b = id >> 4, grp = br * 16 + b;
  int rot7 = b % 7;
  int rg = w & 3, hf = w >> 2;
  STAMP(0);
  const char* kg = (br ? kB : kA) + (size_t)(b * HKV + kvh) * (SK * DH * 2);
  const char* vg = (br ? vB : vA) + (size_t)(b * HKV + kvh) * (SK * DH * 2);
  const char* wog = (br ? woB : woA) + (size_t)kvh * 512 * (DM * 2);

  auto k_eighth = [&](int t, int e) {
    const int ln = lane_id(), hi = ln >> 5;
    const char* kb = kg; SOPAQUE(kb);
    dma4s(lds0 + (t & 3) * SS + e * 1024, kb + (size_t)t * SS + e * 1024, 8192, hi * 512 + (((ln & 31) ^ (2 * e + hi)) << 4));
  };

  int vrot = (id >> 3) & 3;
  auto issue_vw = [&](int p) {
    const unsigned dst = lds0 + (p & 3) * SS + w * 1024;
    const int ln = lane_id();
    const bool isv = p < 20 || (p < 44 && !(p & 1));
    if (isv) {
      const int n = p < 20 ? p - 16 : 4 + ((p - 20) >> 1);
      const int r8 = ln >> 3;
      const char* vb = vg; SOPAQUE(vb);
      dma4s(dst, vb + (size_t)((n & 3) * 256 + 8 * w) * 512 + (((n >> 2) + vrot) & 3) * 128, 64 * 512, r8 * 512 + (((ln & 7) ^ (r8 & 6)) << 4));
    } else {
      const int k = p < 44 ? (p - 20) >> 1 : p - 32;
      int c = ln ^ (w << 1); c = c < 56 ? c : c - 16;
      const char* wb = wog; SOPAQUE(wb);
      dma4s_keep(dst, wb + (size_t)(((k >> 1) & 1) * 256 + (((k >> 2) + vrot) & 3) * 64 + (k & 1) * 32 + w) * 896, 8 * 896, c << 4);
    }
  };
  auto issue = [&](int p) { if (p < 16) k_eighth(p, w); else issue_vw(p); };

  u32x4 pst[16][2];
  f32x4 oa[4][2];
  bf16x8 carry[RDEF];
  float mref[2] = {-INFINITY, -INFINITY};
  float cpk;

  auto vaddr = [&](int slot) {
    const int ln = lane_id(), rl = ((ln >> 4) << 2) | ((ln >> 2) & 3);
    unsigned a = lds0 + slot * SS + hf * 4096 + rl * 128 + ((((ln >> 1) & 1) ^ (rl & 6) ^ (hf << 2)) << 4) + (ln & 1) * 8;
    OPAQUE(a);
    return a;
  };
  auto vrd = [&](unsigned vb, int f) { const unsigned a = (vb ^ (unsigned)((f & 3) << 5)) + (f >> 2) * 8192; return cat8(ldstr(a), ldstr(a + 2048)); };
  auto vmm = [&](auto cc, int f, bf16x8 a) {
    constexpr int c = decltype(cc)::value;
    const bf16x8 b0 = __builtin_bit_cast(bf16x8, pst[4 * c + (f >> 2)][0]), b1 = __builtin_bit_cast(bf16x8, pst[4 * c + (f >> 2)][1]);
    if (c == 0 && f < 4) mm2z(oa[f & 3][0], oa[f & 3][1], a, b0, b1); else mm2(oa[f & 3][0], oa[f & 3][1], a, b0, b1);
  };
  auto nop = []() {};

  for (int attempt = 0;; ++attempt) {
  SOPAQUE(w); SOPAQUE(rot7); SOPAQUE(vrot); rg = w & 3; hf = w >> 2;
  if (w >= 4) {
    const int lw = w - 4, lane = lane_id();
    const char* xg = (br ? xB : xA) + (size_t)b * (SQ * DM * 2); SOPAQUE(xg);
    const char* wqg = (br ? wqB : wqA) + (size_t)kvh * 1024; SOPAQUE(wqg);
    unsigned wofs[4];
#pragma unroll
    for (int j = 0; j < 4; ++j) wofs[j] = j * (8192 - 1024) + ((lane ^ (j << 2)) << 4);
    int wsl = 1;
    auto wtile = [&](int i) {
      int G = (i >> 1) + rot7; G = G >= 7 ? G - 7 : G;
      const char* src = wqg + (size_t)(G * 64 + (i & 1) * 32 + 8 * lw) * 8192;
      const unsigned dst = lds0 + wsl * SS + lw * 8192;
      dma4(dst, src, wofs[0], wofs[1], wofs[2], wofs[3]);
      dma4(dst + 4096, src + 4 * 8192, wofs[0], wofs[1], wofs[2], wofs[3]);
      wsl = wsl == 2 ? 0 : wsl + 1;
    };
    const int xr = 16 * lw + (lane >> 3), xc = lane & 7;
    const unsigned x0 = xr * 896 + ((xc ^ ((xr >> 1) & 7)) << 4), x1 = (xr + 8) * 896 + ((xc ^ (((xr + 8) >> 1) & 7)) << 4) - 1024;
    auto xtile = [&](int Gi) { int G = Gi + rot7; G = G >= 7 ? G - 7 : G; dma2(lds0 + XIMG + Gi * 8192 + lw * 2048, xg + G * 128, x0, x1); };
    xtile(0); wtile(0); wtile(1);
    for (int i = 0; i < 14; ++i) {
      if (i == 1) wbar<20>(); else wbar<8>();
      if (i == 0) for (int Gi = 1; Gi < 7; ++Gi) xtile(Gi);
      if (i + 2 < 14) wtile(i + 2); else { k_eighth(i - 12, 2 * lw); k_eighth(i - 12, 2 * lw + 1); }
    }
    bar();
  } else {
    const int lane = lane_id(), g = lane >> 4, qq = (lane >> 2) & 3, pp = lane & 3;
    const int mh = w & 1, hh = w >> 1, h = lane >> 5, m = lane & 31;
    f32x16 qa[8];
#pragma unroll
    for (int a = 0; a < 8; ++a) qa[a] = (f32x16)(0.f);
    const unsigned wbase = lds0 + (8 * h + qq) * 1024 + hh * 512 + (((2 * (g & 1) + (pp >> 1)) ^ (qq << 2)) << 4) + (pp & 1) * 8;
    const unsigned xbase = lds0 + XIMG + (32 * mh + m) * 128 + ((h ^ ((m >> 1) & 7)) << 4);
    bf16x8 wfA[8], wfB[8], xa, xb, xo;
#pragma unroll
    for (int a = 0; a < 8; ++a) wfB[a] = (bf16x8)(0);
    xo = (bf16x8)(0);
    int sl = SS;
    for (int i = 0; i < 14; ++i) {
      CBAR();
      unsigned p = wbase + sl; OPAQUE(p);
      sl = sl == 2 * SS ? 0 : sl + SS;
      const unsigned xp = (xbase + (i >> 1) * 8192) ^ (unsigned)((i & 1) << 6);
      xa = ldsb128(xp); xb = ldsb128(xp ^ 32);
      SBAR();
#pragma unroll
      for (int a = 0; a < 8; ++a) {
        const unsigned q = (p ^ (unsigned)((a & 3) << 6)) + (a >> 2) * 256; wfA[a] = cat8(ldstr(q), ldstr(q + 4096));
        qa[a] = MFMA32(wfB[a], xo, qa[a]);
        SBAR();
      }
#pragma unroll
      for (int a = 0; a < 8; ++a) {
        if (a < 4) {
#pragma unroll
          for (int u = 0; u < 2; ++u) { const int a2 = 2 * a + u; const unsigned q = (p ^ (unsigned)((a2 & 3) << 6)) + (a2 >> 2) * 256; wfB[a2] = cat8(ldstr(q + 16384), ldstr(q + 16384 + 4096)); }
        }
        qa[a] = MFMA32(wfA[a], xa, qa[a]);
        SBAR();
      }
      xo = xb;
    }
    CBAR();
    const unsigned qw = lds0 + QST + (hh * 2 + mh) * 16384 + (m >> 4) * 8192 + (m & 15) * 16 + h * 8;
#pragma unroll
    for (int nb = 0; nb < 8; ++nb) {
      qa[nb] = MFMA32(wfB[nb], xo, qa[nb]);
#pragma unroll
      for (int a = 0; a < 4; ++a)
        *(LAS u32x2*)(uintptr_t)(qw + nb * 1024 + a * 256) =
            u32x2{cvt2(qa[nb][4 * a] * CEXP, qa[nb][4 * a + 1] * CEXP), cvt2(qa[nb][4 * a + 2] * CEXP, qa[nb][4 * a + 3] * CEXP)};
    }
  }
  CBAR();
  bf16x8 qf[2][8];
  {
    const unsigned qr = lds0 + QST + rg * 16384 + lane_id() * 16;
#pragma unroll
    for (int qb = 0; qb < 2; ++qb)
#pragma unroll
      for (int s = 0; s < 8; ++s) qf[qb][s] = ldsb128(qr + (qb * 8 + s) * 1024);
  }
  if (attempt == STAMP_ATT) STAMP(1);

    f32x4 sa[2][2];
    f32x4 nm[2];
    float tmax[2] = {0.f, 0.f};
    auto kmm = [&](auto tt, int f, bf16x8 a) {
      constexpr int t = decltype(tt)::value;
      if (f >= 2) mm2(sa[f & 1][0], sa[f & 1][1], a, qf[0][f >> 1], qf[1][f >> 1]);
      else if (t == 0) mm2z(sa[f][0], sa[f][1], a, qf[0][0], qf[1][0]);
      else mm2c(sa[f][0], sa[f][1], a, qf[0][0], qf[1][0], nm[0], nm[1]);
    };
    auto smax = [&](int qb) {
      return fmaxf(fmaxf(fmaxf(sa[0][qb][0], sa[0][qb][1]), fmaxf(sa[0][qb][2], sa[0][qb][3])), fmaxf(fmaxf(sa[1][qb][0], sa[1][qb][1]), fmaxf(sa[1][qb][2], sa[1][qb][3])));
    };
    auto soft = [&](auto tt) {
      constexpr int t = decltype(tt)::value;
      MFMA_DRAIN();
#pragma unroll
      for (int qb = 0; qb < 2; ++qb) {
        if (t == 0) {
          mref[qb] = fmaxf(mref[qb], xmax(smax(qb)));
          nm[qb] = (f32x4)(-mref[qb]); OPAQUE(nm[qb]);
          sa[0][qb] += nm[qb]; sa[1][qb] += nm[qb];
        } else if (attempt == 1) tmax[qb] = fmaxf(tmax[qb], smax(qb));
        float e[8];
#pragma unroll
        for (int r = 0; r < 8; ++r) e[r] = __builtin_amdgcn_exp2f(sa[r >> 2][qb][r & 3]);
        pst[t][qb] = u32x4{cvt2(e[0], e[1]), cvt2(e[2], e[3]), cvt2(e[4], e[5]), cvt2(e[6], e[7])};
        OPAQUE(pst[t][qb]);
      }
    };
    auto kstep = [&](auto tt) {
      constexpr int t = decltype(tt)::value;
      const int ln = lane_id();
      unsigned kb0 = lds0 + (t & 3) * SS + hf * 16384 + (ln & 15) * 512 + (((ln >> 4) ^ (ln & 15)) << 4); OPAQUE(kb0);
      if (t == 0) wbar<8>(); else wbar<4>();
      if (hf == 1) issue(t + 2);
      ring_step<16, NBK, 2>(carry,
          [&](int f) { return ldsb128((kb0 ^ (unsigned)(((f >> 1) & 3) << 6)) + (f >> 3) * 256 + (f & 1) * 8192); },
          [&](int f, bf16x8 a) { kmm(IC<t>{}, f, a); },
          [&](int r, bf16x8 a) { if constexpr (t > 0) kmm(IC<t - 1>{}, 16 - RDEF + r, a); },
          [&]() { if constexpr (t > 0) soft(IC<t - 1>{}); });
      if (hf == 0) issue(t + 2);
    };
    kstep(IC<0>{}); kstep(IC<1>{}); kstep(IC<2>{}); kstep(IC<3>{}); kstep(IC<4>{}); kstep(IC<5>{}); kstep(IC<6>{}); kstep(IC<7>{});
    kstep(IC<8>{}); kstep(IC<9>{}); kstep(IC<10>{}); kstep(IC<11>{}); kstep(IC<12>{}); kstep(IC<13>{}); kstep(IC<14>{}); kstep(IC<15>{});
    if (attempt == STAMP_ATT) STAMP(2);
    f32x4 la[2];
    bf16x8 ones;
#pragma unroll
    for (int j = 0; j < 8; ++j) ones[j] = 0x3f80;
    auto lmm = [&](auto cc) {
      constexpr int c = decltype(cc)::value;
#pragma unroll
      for (int u = 0; u < 4; ++u)
#pragma unroll
        for (int qb = 0; qb < 2; ++qb) la[qb] = MFMA16(ones, __builtin_bit_cast(bf16x8, pst[4 * c + u][qb]), (c == 0 && u == 0) ? (f32x4)(0.f) : la[qb]);
    };
    {
      const unsigned vb = vaddr(0); wbar<4>(); if (hf == 1) { issue_vw(18); issue_vw(19); }
      ring_step<16, NBV, 2>(carry, [&](int f) { return vrd(vb, f); }, [&](int f, bf16x8 a) { vmm(IC<0>{}, f, a); },
                         [&](int r, bf16x8 a) { kmm(IC<15>{}, 16 - RDEF + r, a); }, [&]() { soft(IC<15>{}); lmm(IC<0>{}); });
      if (hf == 0) { issue_vw(18); issue_vw(19); }
    }
    {
      const unsigned vb = vaddr(1); wbar<8>(); if (hf == 1) issue_vw(20);
      ring_step<16, NBV, 2>(carry, [&](int f) { return vrd(vb, f); }, [&](int f, bf16x8 a) { vmm(IC<1>{}, f, a); },
                         [&](int r, bf16x8 a) { vmm(IC<0>{}, 16 - RDEF + r, a); }, [&]() { lmm(IC<1>{}); });
      if (hf == 0) issue_vw(20);
    }
    {
      const unsigned vb = vaddr(2); wbar<8>(); if (hf == 1) issue_vw(21);
      ring_step<16, NBV, 2>(carry, [&](int f) { return vrd(vb, f); }, [&](int f, bf16x8 a) { vmm(IC<2>{}, f, a); },
                         [&](int r, bf16x8 a) { vmm(IC<1>{}, 16 - RDEF + r, a); }, [&]() { lmm(IC<2>{}); lmm(IC<3>{}); });
      const int ln = lane_id();
      const bool bad = !(la[0][0] <= LMAX) || !(la[1][0] <= LMAX);
      const float anybad = __any(bad) ? 1.f : 0.f;
      if (ln < 16) *(LAS f32x4*)(uintptr_t)(lds0 + MISC + w * 256 + ln * 16) = f32x4{mref[0], la[0][0], mref[1], la[1][0]};
      if (ln == 0) *(LAS float*)(uintptr_t)(lds0 + MISC + 2048 + w * 4) = anybad;
      if (hf == 0) issue_vw(21);
    }
    float fl;
    {
      const unsigned vb = vaddr(3); wbar<8>(); if (hf == 1) issue_vw(22);
      ring_step<16, NBV, 2>(carry, [&](int f) { return vrd(vb, f); }, [&](int f, bf16x8 a) { vmm(IC<3>{}, f, a); },
                         [&](int r, bf16x8 a) { vmm(IC<2>{}, 16 - RDEF + r, a); },
                         [&]() {
                           const int ln = lane_id();
                           const f32x4 o = *(const LAS f32x4*)(uintptr_t)(lds0 + MISC + (w ^ 4) * 256 + (ln & 15) * 16);
                           const f32x4 f0 = *(const LAS f32x4*)(uintptr_t)(lds0 + MISC + 2048), f1 = *(const LAS f32x4*)(uintptr_t)(lds0 + MISC + 2048 + 16);
                           fl = (f0[0] + f0[1]) + (f0[2] + f0[3]) + (f1[0] + f1[1]) + (f1[2] + f1[3]); OPAQUE(fl);
                           const bool odd = (ln >> 4) & 1;
                           float l0 = la[0][0], l1 = la[1][0], m0 = mref[0], m1 = mref[1], o0_ = o[0], o1_ = o[1], o2_ = o[2], o3_ = o[3];
                           OPAQUE(l0); OPAQUE(l1); OPAQUE(m0); OPAQUE(m1); OPAQUE(o0_); OPAQUE(o1_); OPAQUE(o2_); OPAQUE(o3_);
                           const float ms = odd ? m1 : m0, ls = odd ? l1 : l0, mo = odd ? o2_ : o0_, lo = odd ? o3_ : o1_;
                           const float m = fmaxf(ms, mo), ea = __builtin_amdgcn_exp2f(ms - m), eb = __builtin_amdgcn_exp2f(mo - m);
                           cpk = ea / (ls * ea + lo * eb); OPAQUE(cpk);
                         });
      if (hf == 0) issue_vw(22);
    }
    if (attempt == 2 || (attempt == 0 && __builtin_amdgcn_readfirstlane(__float_as_uint(fl)) == 0u)) break;
    mref[0] += xmax(tmax[0]); mref[1] += xmax(tmax[1]);
    wbar<0>();
  }

  const int qh = w >> 2, nq = w & 3;
  f32x4 pa[7][2];
#pragma unroll
  for (int i = 0; i < 7; ++i) { pa[i][0] = (f32x4)(0.f); pa[i][1] = (f32x4)(0.f); }
  bf16x8 o0 = (bf16x8)(0), o1 = (bf16x8)(0);
  auto oload = [&](int idx) {
    const unsigned oad = lds0 + MISC + ((((idx >> 1) * 2 + qh) + 4 * ((idx & 1) ^ 1)) * 4096) + lane_id() * 16;
    o0 = ldsb128(oad); o1 = ldsb128(oad + 1024);
  };
  auto wmm = [&](int i, bf16x8 a) { mm2(pa[i][0], pa[i][1], a, o0, o1); };
  auto comb_write = [&]() {
    MFMA_DRAIN();
    const int ln = lane_id();
    float cc = cpk; OPAQUE(cc);
    const auto cr = __builtin_amdgcn_permlane16_swap(__float_as_uint(cc), __float_as_uint(cc), false, false);
    const float c0 = __uint_as_float(cr[0]), c1 = __uint_as_float(cr[1]);
#pragma unroll
    for (int db = 0; db < 4; ++db) { oa[db][0] *= c0; oa[db][1] *= c1; }
#pragma unroll
    for (int k = 0; k < 2; ++k)
#pragma unroll
      for (int qb = 0; qb < 2; ++qb) *(LAS f32x4*)(uintptr_t)(lds0 + MISC + w * 4096 + (k * 2 + qb) * 1024 + ln * 16) = oa[2 + k][qb];
  };
  auto comb_read = [&]() {
    const unsigned pad = lds0 + MISC + (w ^ 4) * 4096 + lane_id() * 16;
    u32x4 f[2];
#pragma unroll
    for (int qb = 0; qb < 2; ++qb) {
      const f32x4 a = *(const LAS f32x4*)(uintptr_t)(pad + qb * 1024) + oa[0][qb];
      const f32x4 c = *(const LAS f32x4*)(uintptr_t)(pad + (2 + qb) * 1024) + oa[1][qb];
      f[qb] = u32x4{cvt2(a[0], a[1]), cvt2(a[2], a[3]), cvt2(c[0], c[1]), cvt2(c[2], c[3])};
    }
    *(LAS u32x4*)(uintptr_t)(pad) = f[0];
    *(LAS u32x4*)(uintptr_t)(pad + 1024) = f[1];
  };
  auto vstep = [&](int slot, int pn, auto cc, auto mid) {
    const unsigned vb = vaddr(slot);
    wbar<8>(); TS(decltype(cc)::value == 0 ? 2 : 1);
    if (hf == 1) issue_vw(pn);
    ring_step<16, NBV, 2>(carry, [&](int f) { return vrd(vb, f); }, [&](int f, bf16x8 a) { vmm(cc, f, a); },
                       [&](int r, bf16x8 a) { wmm(7 - RDEF + r, a); }, mid);
  };
  auto wstep = [&](int slot, auto pre, auto prev, auto mid) {
    const int ln = lane_id(), rl = ((ln >> 4) << 2) | ((ln >> 2) & 3);
    unsigned wb = lds0 + slot * SS + rl * 1024 + (ln & 1) * 8;
    unsigned cx = ((ln >> 1) & 1) ^ ((rl & 7) << 1);
    PIN2(wb, cx);
    pre();
    ring_step<7, NBW, NBW - RDEF>(carry, [&](int i) { const unsigned a = wb + (((unsigned)(14 * nq + 2 * i) ^ cx) << 4); return cat8(ldstr(a), ldstr(a + 16384)); },
                      wmm, prev, mid);
  };
#pragma unroll
  for (int r = 0; r < RDEF; ++r) { vmm(IC<3>{}, 16 - RDEF + r, carry[r]); carry[r] = (bf16x8)(0); }
  CBAR();
  comb_write();
#pragma unroll 1
  for (int p = 20; p < 44; p += 8) {
    vstep(0, p + 3, IC<0>{}, comb_read); if (hf == 0) issue_vw(p + 3);
    wstep(1, [&]() { wbar<8>(); TS(3); if (hf == 1) issue_vw(p + 4); oload(0); }, [&](int r, bf16x8 a) { vmm(IC<0>{}, 16 - RDEF + r, a); }, nop); if (hf == 0) issue_vw(p + 4);
    vstep(2, p + 5, IC<1>{}, nop); if (hf == 0) issue_vw(p + 5);
    wstep(3, [&]() { wbar<8>(); TS(0); if (hf == 1) issue_vw(p + 6); oload(1); }, [&](int r, bf16x8 a) { vmm(IC<1>{}, 16 - RDEF + r, a); }, nop); if (hf == 0) issue_vw(p + 6);
    vstep(0, p + 7, IC<2>{}, nop); if (hf == 0) issue_vw(p + 7);
    wstep(1, [&]() { wbar<8>(); TS(0); if (hf == 1) issue_vw(p + 8); oload(2); }, [&](int r, bf16x8 a) { vmm(IC<2>{}, 16 - RDEF + r, a); }, nop); if (hf == 0) issue_vw(p + 8);
    vstep(2, p + 9, IC<3>{}, nop); oload(3); if (hf == 0) issue_vw(p + 9);
    wstep(3, [&]() { wbar<8>(); TS(0); if (hf == 1) issue_vw(p + 10); }, [&](int r, bf16x8 a) { vmm(IC<3>{}, 16 - RDEF + r, a); }, comb_write); if (hf == 0) issue_vw(p + 10);
  }
  STAMP(3);
  CBAR();
#pragma unroll
  for (int r = 0; r < RDEF; ++r) wmm(7 - RDEF + r, carry[r]);
  comb_read(); issue_vw(47);
  auto wprev = [&](int r, bf16x8 a) { wmm(7 - RDEF + r, a); };
  unsigned long long* ctl = (unsigned long long*)(ws + (size_t)grp * 2048);
  unsigned long long ep;
  wstep(0, [&]() { wbar<8>(); oload(0); }, [&](int, bf16x8) {}, nop);
  wstep(1, nop, wprev, [&]() { oload(1); });
  wstep(2, [&]() { wbar<0>(); ep = __hip_atomic_load(ctl, __ATOMIC_RELAXED, __HIP_MEMORY_SCOPE_AGENT); }, wprev, [&]() { oload(2); });
  wstep(3, nop, wprev, [&]() { oload(3); });
#pragma unroll
  for (int r = 0; r < RDEF; ++r) wmm(7 - RDEF + r, carry[r]);
  MFMA_DRAIN();
  const int lane = lane_id(), g = lane >> 4, li = lane & 15;
  const __amdgpu_buffer_rsrc_t wsr = __builtin_amdgcn_make_buffer_rsrc(ws, 0, 0x7fffffff, 0x00020000);
#pragma unroll
  for (int i = 0; i < 7; ++i) {
    u32x4 f = {cvt2(pa[i][0][0], pa[i][0][1]), cvt2(pa[i][0][2], pa[i][0][3]), cvt2(pa[i][1][0], pa[i][1][1]), cvt2(pa[i][1][2], pa[i][1][3])};
    const unsigned off = (unsigned)WS_PART + (((unsigned)(grp * 56 + w * 7 + i) * 8 + kvh) << 10) + lane * 16;
    __builtin_amdgcn_raw_buffer_store_b128(f, wsr, off, 0, 16);
  }
  const unsigned long long tag = ep * 0x9E3779B97F4A7C15ull + 0x632BE59BD9B4E019ull;
  STAMP(4);
  wbar<0>();
  STAMP(5);
  if (w == 0 && lane == 0) __hip_atomic_store(ctl + 16 * (kvh + 1), tag, __ATOMIC_RELAXED, __HIP_MEMORY_SCOPE_AGENT);
  if (w < 7) {
    for (unsigned spin = 0; spin < (1u << 22); ++spin) {
      unsigned long long f = tag;
      if (lane < 8) f = __hip_atomic_load(ctl + 16 * (lane + 1), __ATOMIC_RELAXED, __HIP_MEMORY_SCOPE_AGENT);
      if (__all(f == tag)) break;
      __builtin_amdgcn_s_sleep(2);
    }
    __builtin_amdgcn_fence(__ATOMIC_ACQUIRE, "wavefront");
    STAMP(6);
    const unsigned off = (unsigned)WS_PART + ((unsigned)(grp * 56 + kvh * 7 + w) << 13) + lane * 16;
    u32x4 v[8];
#pragma unroll
    for (int s = 0; s < 8; ++s) v[s] = __builtin_amdgcn_raw_buffer_load_b128(wsr, off + s * 1024, 0, 16);
    float acc[8];
#pragma unroll
    for (int e = 0; e < 8; ++e) acc[e] = 0.f;
#pragma unroll
    for (int s = 0; s < 8; ++s)
#pragma unroll
      for (int e = 0; e < 4; ++e) { acc[2 * e] += __uint_as_float(v[s][e] << 16); acc[2 * e + 1] += __uint_as_float(v[s][e] & 0xffff0000u); }
    const int q = 32 * (kvh >> 2) + li, n = 112 * (kvh & 3) + 16 * w + 4 * g;
    char* o = out + ((size_t)(b * SQ + q) * (2 * DM) + br * DM + n) * 2;
    *(u32x2*)o = u32x2{cvt2(acc[0], acc[1]), cvt2(acc[2], acc[3])};
    *(u32x2*)(o + 16 * 2 * DM * 2) = u32x2{cvt2(acc[4], acc[5]), cvt2(acc[6], acc[7])};
    STAMP(7);
    if (kvh == 0 && w == 0 && lane == 0) __hip_atomic_store(ctl, tag, __ATOMIC_RELAXED, __HIP_MEMORY_SCOPE_AGENT);
  }
}

extern "C" void kernel_launch(void* const* d_in, const int* in_sizes, int n_in, void* d_out, int out_size, void* d_ws, size_t ws_size,
                              hipStream_t stream) {
  (void)in_sizes; (void)n_in; (void)out_size; (void)ws_size;
  (void)hipFuncSetAttribute((const void*)gqa_fused_kernel, hipFuncAttributeMaxDynamicSharedMemorySize, LDS_BYTES);
  gqa_fused_kernel<<<256, 512, LDS_BYTES, stream>>>(
      (const char*)d_in[0], (const char*)d_in[1], (const char*)d_in[2], (const char*)d_in[3], (const char*)d_in[4],
      (const char*)d_in[5], (const char*)d_in[6], (const char*)d_in[7], (const char*)d_in[8], (const char*)d_in[9],
      (char*)d_out, (char*)d_ws);
}
```

```cpp
#include <hip/hip_runtime.h>
#include <hip/hip_bf16.h>
#include <stdint.h>
#include <type_traits>
#define CODE_BYTES 50000u

typedef __attribute__((ext_vector_type(8))) short bf16x8;
typedef __attribute__((ext_vector_type(4))) short s16x4;
typedef __attribute__((ext_vector_type(4))) float f32x4;
typedef __attribute__((ext_vector_type(16))) float f32x16;
typedef __attribute__((ext_vector_type(2))) float f32x2;
typedef __attribute__((ext_vector_type(2))) __bf16 bf16x2_t;
typedef __attribute__((ext_vector_type(4))) unsigned u32x4;
typedef __attribute__((ext_vector_type(2))) unsigned u32x2;
#define LAS __attribute__((address_space(3)))
typedef LAS char* lds_ptr;

constexpr int SQ = 64, DM = 448, HKV = 8, SK = 1024, DH = 256;
constexpr int SS = 32768;
constexpr int MISC = 4 * SS;
constexpr int LDS_BYTES = 5 * SS;
constexpr int XIMG = 3 * SS;
constexpr int QST = 3 * SS;
constexpr int OST = 2 * SS;
constexpr float CEXP = 0.0625f * 1.4426950408889634f;
#define LMAX 1.0e18f
#define STAMP_ATT 0
#define LA 2
#define NBK 5
#define NBV 6
#define NBW 4
constexpr int RDEF = 2;

constexpr size_t WS_PART = 65536;

#define MFMA16(a, b, c) __builtin_amdgcn_mfma_f32_16x16x32_bf16((a), (b), (c), 0, 0, 0)
#define MFMA32(a, b, c) __builtin_amdgcn_mfma_f32_32x32x16_bf16((a), (b), (c), 0, 0, 0)
#define CBAR() asm volatile("s_waitcnt lgkmcnt(0)\n\ts_barrier" ::: "memory")
#define SBAR() __builtin_amdgcn_sched_barrier(0)
#define OPAQUE(x) asm volatile("" : "+v"(x))
#define PIN2(x, y) asm volatile("" : "+v"(x), "+v"(y))
#define SOPAQUE(x) asm volatile("" : "+s"(x))
#define DI __device__ __forceinline__
template <int N> DI void wbar() { asm volatile("s_waitcnt vmcnt(%0) lgkmcnt(0)\n\ts_barrier" ::"i"(N) : "memory"); }
DI void bar() { asm volatile("s_barrier" ::: "memory"); }
template <int N> using IC = std::integral_constant<int, N>;

DI unsigned cvt2(float a, float b) { f32x2 v = {a, b}; return __builtin_bit_cast(unsigned, __builtin_convertvector(v, bf16x2_t)); }
DI bf16x8 ldsb128(unsigned a) { return *(const LAS bf16x8*)(uintptr_t)a; }
DI s16x4 ldstr(unsigned a) { return __builtin_amdgcn_ds_read_tr16_b64_v4i16((LAS s16x4*)(uintptr_t)a); }
DI bf16x8 cat8(s16x4 a, s16x4 b) { return __builtin_shufflevector(a, b, 0, 1, 2, 3, 4, 5, 6, 7); }
DI int lane_id() { int l; asm volatile("v_mbcnt_lo_u32_b32 %0, -1, 0\n\tv_mbcnt_hi_u32_b32 %0, -1, %0" : "=v"(l)); return l; }
DI float xmax(float v) {
  { auto r = __builtin_amdgcn_permlane16_swap(__float_as_uint(v), __float_as_uint(v), false, false); v = fmaxf(__uint_as_float(r[0]), __uint_as_float(r[1])); }
  { auto r = __builtin_amdgcn_permlane32_swap(__float_as_uint(v), __float_as_uint(v), false, false); v = fmaxf(__uint_as_float(r[0]), __uint_as_float(r[1])); }
  return v;
}

DI void mm2(f32x4& c0, f32x4& c1, bf16x8 a, bf16x8 b0, bf16x8 b1) {
  asm volatile("v_mfma_f32_16x16x32_bf16 %0, %2, %3, %0\n\tv_mfma_f32_16x16x32_bf16 %1, %2, %4, %1" : "+v"(c0), "+v"(c1) : "v"(a), "v"(b0), "v"(b1));
}
DI void mm2z(f32x4& c0, f32x4& c1, bf16x8 a, bf16x8 b0, bf16x8 b1) {
  asm volatile("v_mfma_f32_16x16x32_bf16 %0, %2, %3, 0\n\tv_mfma_f32_16x16x32_bf16 %1, %2, %4, 0" : "=&v"(c0), "=&v"(c1) : "v"(a), "v"(b0), "v"(b1));
}
DI void mm2c(f32x4& c0, f32x4& c1, bf16x8 a, bf16x8 b0, bf16x8 b1, f32x4 i0, f32x4 i1) {
  asm volatile("v_mfma_f32_16x16x32_bf16 %0, %2, %3, %5\n\tv_mfma_f32_16x16x32_bf16 %1, %2, %4, %6" : "=&v"(c0), "=&v"(c1) : "v"(a), "v"(b0), "v"(b1), "v"(i0), "v"(i1));
}
#define MFMA_DRAIN() do { asm volatile("s_nop 7\n\ts_nop 1" ::: "memory"); SBAR(); } while (0)

DI void dma4(unsigned lds, const void* sbase, unsigned v0, unsigned v1, unsigned v2, unsigned v3) {
  unsigned keep;
  asm volatile("s_mov_b32 %0, m0\n\ts_mov_b32 m0, %1\n\ts_nop 3\n\t"
               "global_load_lds_dwordx4 %2, %6\n\tglobal_load_lds_dwordx4 %3, %6 offset:1024\n\t"
               "global_load_lds_dwordx4 %4, %6 offset:2048\n\tglobal_load_lds_dwordx4 %5, %6 offset:3072\n\ts_mov_b32 m0, %0"
               : "=&s"(keep) : "s"(lds), "v"(v0), "v"(v1), "v"(v2), "v"(v3), "s"(sbase) : "memory");
}
DI void dma2(unsigned lds, const void* sbase, unsigned v0, unsigned v1) {
  unsigned keep;
  asm volatile("s_mov_b32 %0, m0\n\ts_mov_b32 m0, %1\n\ts_nop 3\n\t"
               "global_load_lds_dwordx4 %2, %4\n\tglobal_load_lds_dwordx4 %3, %4 offset:1024\n\ts_mov_b32 m0, %0"
               : "=&s"(keep) : "s"(lds), "v"(v0), "v"(v1), "s"(sbase) : "memory");
}
DI void dma4s(unsigned lds, const char* s, size_t gstride, unsigned v) {
  unsigned keep;
  asm volatile("s_mov_b32 %0, m0\n\ts_mov_b32 m0, %1\n\ts_nop 3\n\tglobal_load_lds_dwordx4 %2, %3 nt\n\t"
               "s_add_u32 m0, m0, 0x2000\n\ts_nop 0\n\tglobal_load_lds_dwordx4 %2, %4 nt\n\t"
               "s_add_u32 m0, m0, 0x2000\n\ts_nop 0\n\tglobal_load_lds_dwordx4 %2, %5 nt\n\t"
               "s_add_u32 m0, m0, 0x2000\n\ts_nop 0\n\tglobal_load_lds_dwordx4 %2, %6 nt\n\ts_mov_b32 m0, %0"
               : "=&s"(keep) : "s"(lds), "v"(v), "s"(s), "s"(s + gstride), "s"(s + 2 * gstride), "s"(s + 3 * gstride) : "memory", "scc");
}

DI void dma4s_keep(unsigned lds, const char* s, size_t gstride, unsigned v) {
  unsigned keep;
  asm volatile("s_mov_b32 %0, m0\n\ts_mov_b32 m0, %1\n\ts_nop 3\n\tglobal_load_lds_dwordx4 %2, %3\n\t"
               "s_add_u32 m0, m0, 0x2000\n\ts_nop 0\n\tglobal_load_lds_dwordx4 %2, %4\n\t"
               "s_add_u32 m0, m0, 0x2000\n\ts_nop 0\n\tglobal_load_lds_dwordx4 %2, %5\n\t"
               "s_add_u32 m0, m0, 0x2000\n\ts_nop 0\n\tglobal_load_lds_dwordx4 %2, %6\n\ts_mov_b32 m0, %0"
               : "=&s"(keep) : "s"(lds), "v"(v), "s"(s), "s"(s + gstride), "s"(s + 2 * gstride), "s"(s + 3 * gstride) : "memory", "scc");
}

template <int NF, int NB, int NI, class RD, class MM, class PREV, class MID>
DI void ring_step(bf16x8 (&carry)[RDEF], RD rd, MM mm, PREV prev, MID mid) {
  static_assert(NI + RDEF <= NB, "");
  bf16x8 fb[NB];
#pragma unroll
  for (int f = 0; f < NI; ++f) fb[f] = rd(f);
  SBAR();
#pragma unroll
  for (int r = 0; r < RDEF; ++r) { prev(r, carry[r]); SBAR(); fb[NI + r] = rd(NI + r); SBAR(); }
  mid();
  SBAR();
#pragma unroll
  for (int f = NI + RDEF; f < NB; ++f) fb[f] = rd(f);
  SBAR();
#pragma unroll
  for (int f = 0; f < NF - RDEF; ++f) {
    mm(f, fb[f % NB]); SBAR();
    if (f + NB < NF) { fb[f % NB] = rd(f + NB); SBAR(); }
  }
#pragma unroll
  for (int r = 0; r < RDEF; ++r) carry[r] = fb[(NF - RDEF + r) % NB];
}

#define TS(k) do {} while (0)
#define STAMP(i) do {} while (0)

__global__ __launch_bounds__(512, 2) void gqa_fused_kernel(
    const char* __restrict__ xA, const char* __restrict__ kA, const char* __restrict__ vA, const char* __restrict__ wqA, const char* __restrict__ woA,
    const char* __restrict__ xB, const char* __restrict__ kB, const char* __restrict__ vB, const char* __restrict__ wqB, const char* __restrict__ woB,
    char* __restrict__ out, char* __restrict__ ws) {
  extern __shared__ __attribute__((aligned(16))) char smem[];
  const unsigned lds0 = (unsigned)(uintptr_t)(lds_ptr)smem;
  int w = __builtin_amdgcn_readfirstlane(threadIdx.x >> 6);
  const int id = blockIdx.x, br = (id >> 2) & 1, kvh = 2 * (id & 3) + ((id >> 3) & 1), b = id >> 4, grp = br * 16 + b;
  int rot7 = b % 7;
  int rg = w & 3, hf = w >> 2;
  STAMP(0);
  unsigned long long pc0; asm volatile("s_getpc_b64 %0" : "=s"(pc0));
  const char* kg = (br ? kB : kA) + (size_t)(b * HKV + kvh) * (SK * DH * 2);
  const char* vg = (br ? vB : vA) + (size_t)(b * HKV + kvh) * (SK * DH * 2);
  const char* wog = (br ? woB : woA) + (size_t)kvh * 512 * (DM * 2);

  auto k_eighth = [&](int t, int e) {
    const int ln = lane_id(), hi = ln >> 5;
    const char* kb = kg; SOPAQUE(kb);
    dma4s(lds0 + (t & 3) * SS + e * 1024, kb + (size_t)t * SS + e * 1024, 8192, hi * 512 + (((ln & 31) ^ (2 * e + hi)) << 4));
  };

  int vrot = (id >> 3) & 3;
  auto wo_eighth = [&](int T, unsigned base, int e) {
    const int ln = lane_id(), r8 = ln >> 3;
    int r = (T >> 1) + rot7; r = r >= 7 ? r - 7 : r;
    const char* wb = wog; SOPAQUE(wb);
    dma4s_keep(lds0 + base + e * 1024, wb + (size_t)((T & 1) * 256 + 8 * e) * 896 + r * 128, 64 * 896, r8 * 896 + (((ln & 7) ^ ((r8 & 2) << 1)) << 4));
  };
  auto issue = [&](int p) {
    if (p < 16) k_eighth(p, w);
    else if (p < 32) {
      const int ln = lane_id(), n = p - 16, r8 = ln >> 3;
      const char* vb = vg; SOPAQUE(vb);
      dma4s(lds0 + (p & 3) * SS + w * 1024, vb + (size_t)((n & 3) * 256 + 8 * w) * 512 + (((n >> 2) + vrot) & 3) * 128, 64 * 512, r8 * 512 + (((ln & 7) ^ (r8 & 6)) << 4));
    } else if (p < 34) wo_eighth(p - 32, (p & 3) * SS, w);
  };

  u32x4 pst[16][2];
  f32x4 oa[4][2];
  bf16x8 carry[RDEF];
  float mref[2] = {-INFINITY, -INFINITY};
  float cpk;

  auto vaddr = [&](int slot) {
    const int ln = lane_id(), rl = ((ln >> 4) << 2) | ((ln >> 2) & 3);
    unsigned a = lds0 + slot * SS + hf * 4096 + rl * 128 + ((((ln >> 1) & 1) ^ (rl & 6) ^ (hf << 2)) << 4) + (ln & 1) * 8;
    OPAQUE(a);
    return a;
  };
  auto vrd = [&](unsigned vb, int f) { const unsigned a = (vb ^ (unsigned)((f & 3) << 5)) + (f >> 2) * 8192; return cat8(ldstr(a), ldstr(a + 2048)); };
  auto vmm = [&](auto cc, int f, bf16x8 a) {
    constexpr int c = decltype(cc)::value;
    const bf16x8 b0 = __builtin_bit_cast(bf16x8, pst[4 * c + (f >> 2)][0]), b1 = __builtin_bit_cast(bf16x8, pst[4 * c + (f >> 2)][1]);
    if (c == 0 && f < 4) mm2z(oa[f & 3][0], oa[f & 3][1], a, b0, b1); else mm2(oa[f & 3][0], oa[f & 3][1], a, b0, b1);
  };
  auto nop = []() {};

  for (int attempt = 0;; ++attempt) {
  SOPAQUE(w); SOPAQUE(rot7); SOPAQUE(vrot); rg = w & 3; hf = w >> 2;
  if (w >= 4) {
    const int lw = w - 4, lane = lane_id();
    const char* xg = (br ? xB : xA) + (size_t)b * (SQ * DM * 2); SOPAQUE(xg);
    const char* wqg = (br ? wqB : wqA) + (size_t)kvh * 1024; SOPAQUE(wqg);
    unsigned wofs[4];
#pragma unroll
    for (int j = 0; j < 4; ++j) wofs[j] = j * (8192 - 1024) + ((lane ^ (j << 2)) << 4);
    int wsl = 1;
    auto wtile = [&](int i) {
      int G = (i >> 1) + rot7; G = G >= 7 ? G - 7 : G;
      const char* src = wqg + (size_t)(G * 64 + (i & 1) * 32 + 8 * lw) * 8192;
      const unsigned dst = lds0 + wsl * SS + lw * 8192;
      dma4(dst, src, wofs[0], wofs[1], wofs[2], wofs[3]);
      dma4(dst + 4096, src + 4 * 8192, wofs[0], wofs[1], wofs[2], wofs[3]);
      wsl = wsl == 2 ? 0 : wsl + 1;
    };
    const int xr = 16 * lw + (lane >> 3), xc = lane & 7;
    const unsigned x0 = xr * 896 + ((xc ^ ((xr >> 1) & 7)) << 4), x1 = (xr + 8) * 896 + ((xc ^ (((xr + 8) >> 1) & 7)) << 4) - 1024;
    auto xtile = [&](int Gi) { int G = Gi + rot7; G = G >= 7 ? G - 7 : G; dma2(lds0 + XIMG + Gi * 8192 + lw * 2048, xg + G * 128, x0, x1); };
    xtile(0); wtile(0); wtile(1);
    for (int i = 0; i < 14; ++i) {
      if (i == 1) wbar<20>(); else wbar<8>();
      if (i == 0) for (int Gi = 1; Gi < 7; ++Gi) xtile(Gi);
      if (i + 2 < 14) wtile(i + 2); else { k_eighth(i - 12, 2 * lw); k_eighth(i - 12, 2 * lw + 1); }
    }
    bar();
  } else {
    const int lane = lane_id(), g = lane >> 4, qq = (lane >> 2) & 3, pp = lane & 3;
    const int mh = w & 1, hh = w >> 1, h = lane >> 5, m = lane & 31;
    f32x16 qa[8];
#pragma unroll
    for (int a = 0; a < 8; ++a) qa[a] = (f32x16)(0.f);
    const unsigned wbase = lds0 + (8 * h + qq) * 1024 + hh * 512 + (((2 * (g & 1) + (pp >> 1)) ^ (qq << 2)) << 4) + (pp & 1) * 8;
    const unsigned xbase = lds0 + XIMG + (32 * mh + m) * 128 + ((h ^ ((m >> 1) & 7)) << 4);
    bf16x8 wfA[8], wfB[8], xa, xb, xo;
#pragma unroll
    for (int a = 0; a < 8; ++a) wfB[a] = (bf16x8)(0);
    xo = (bf16x8)(0);
    int sl = SS;
    unsigned tch = 0, tch2 = 0;
    asm volatile("global_load_dword %0, %1, %2" : "=v"(tch) : "v"((w * 64 + lane) * 128), "s"(pc0) : "memory");
    { unsigned o = 32768 + (w * 64 + lane) * 128; o = o < CODE_BYTES ? o : 0; asm volatile("global_load_dword %0, %1, %2" : "=v"(tch2) : "v"(o), "s"(pc0) : "memory"); }
    for (int i = 0; i < 14; ++i) {
      CBAR();
      unsigned p = wbase + sl; OPAQUE(p);
      sl = sl == 2 * SS ? 0 : sl + SS;
      const unsigned xp = (xbase + (i >> 1) * 8192) ^ (unsigned)((i & 1) << 6);
      xa = ldsb128(xp); xb = ldsb128(xp ^ 32);
      SBAR();
#pragma unroll
      for (int a = 0; a < 8; ++a) {
        const unsigned q = (p ^ (unsigned)((a & 3) << 6)) + (a >> 2) * 256; wfA[a] = cat8(ldstr(q), ldstr(q + 4096));
        qa[a] = MFMA32(wfB[a], xo, qa[a]);
        SBAR();
      }
#pragma unroll
      for (int a = 0; a < 8; ++a) {
        if (a < 4) {
#pragma unroll
          for (int u = 0; u < 2; ++u) { const int a2 = 2 * a + u; const unsigned q = (p ^ (unsigned)((a2 & 3) << 6)) + (a2 >> 2) * 256; wfB[a2] = cat8(ldstr(q + 16384), ldstr(q + 16384 + 4096)); }
        }
        qa[a] = MFMA32(wfA[a], xa, qa[a]);
        SBAR();
      }
      xo = xb;
    }
    wbar<0>();
    OPAQUE(tch); OPAQUE(tch2);
    const unsigned qw = lds0 + QST + (hh * 2 + mh) * 16384 + (m >> 4) * 8192 + (m & 15) * 16 + h * 8;
#pragma unroll
    for (int nb = 0; nb < 8; ++nb) {
      qa[nb] = MFMA32(wfB[nb], xo, qa[nb]);
#pragma unroll
      for (int a = 0; a < 4; ++a)
        *(LAS u32x2*)(uintptr_t)(qw + nb * 1024 + a * 256) =
            u32x2{cvt2(qa[nb][4 * a] * CEXP, qa[nb][4 * a + 1] * CEXP), cvt2(qa[nb][4 * a + 2] * CEXP, qa[nb][4 * a + 3] * CEXP)};
    }
  }
  CBAR();
  bf16x8 qf[2][8];
  {
    const unsigned qr = lds0 + QST + rg * 16384 + lane_id() * 16;
#pragma unroll
    for (int qb = 0; qb < 2; ++qb)
#pragma unroll
      for (int s = 0; s < 8; ++s) qf[qb][s] = ldsb128(qr + (qb * 8 + s) * 1024);
  }
  if (attempt == STAMP_ATT) STAMP(1);

    f32x4 sa[2][2];
    f32x4 nm[2];
    float tmax[2] = {0.f, 0.f};
    auto kmm = [&](auto tt, int f, bf16x8 a) {
      constexpr int t = decltype(tt)::value;
      if (f >= 2) mm2(sa[f & 1][0], sa[f & 1][1], a, qf[0][f >> 1], qf[1][f >> 1]);
      else if (t == 0) mm2z(sa[f][0], sa[f][1], a, qf[0][0], qf[1][0]);
      else mm2c(sa[f][0], sa[f][1], a, qf[0][0], qf[1][0], nm[0], nm[1]);
    };
    auto smax = [&](int qb) {
      return fmaxf(fmaxf(fmaxf(sa[0][qb][0], sa[0][qb][1]), fmaxf(sa[0][qb][2], sa[0][qb][3])), fmaxf(fmaxf(sa[1][qb][0], sa[1][qb][1]), fmaxf(sa[1][qb][2], sa[1][qb][3])));
    };
    auto soft = [&](auto tt) {
      constexpr int t = decltype(tt)::value;
      MFMA_DRAIN();
#pragma unroll
      for (int qb = 0; qb < 2; ++qb) {
        if (t == 0) {
          mref[qb] = fmaxf(mref[qb], xmax(smax(qb)));
          nm[qb] = (f32x4)(-mref[qb]); OPAQUE(nm[qb]);
          sa[0][qb] += nm[qb]; sa[1][qb] += nm[qb];
        } else if (attempt == 1) tmax[qb] = fmaxf(tmax[qb], smax(qb));
        float e[8];
#pragma unroll
        for (int r = 0; r < 8; ++r) e[r] = __builtin_amdgcn_exp2f(sa[r >> 2][qb][r & 3]);
        pst[t][qb] = u32x4{cvt2(e[0], e[1]), cvt2(e[2], e[3]), cvt2(e[4], e[5]), cvt2(e[6], e[7])};
        OPAQUE(pst[t][qb]);
      }
    };
    auto kstep = [&](auto tt) {
      constexpr int t = decltype(tt)::value;
      const int ln = lane_id();
      unsigned kb0 = lds0 + (t & 3) * SS + hf * 16384 + (ln & 15) * 512 + (((ln >> 4) ^ (ln & 15)) << 4); OPAQUE(kb0);
      if (t == 0) wbar<8>(); else wbar<4 * (LA - 1)>();
      if (LA == 3 && t == 0) issue(2);
      if (hf == 1) issue(t + LA);
      ring_step<16, NBK, 2>(carry,
          [&](int f) { return ldsb128((kb0 ^ (unsigned)(((f >> 1) & 3) << 6)) + (f >> 3) * 256 + (f & 1) * 8192); },
          [&](int f, bf16x8 a) { kmm(IC<t>{}, f, a); },
          [&](int r, bf16x8 a) { if constexpr (t > 0) kmm(IC<t - 1>{}, 16 - RDEF + r, a); },
          [&]() { if constexpr (t > 0) soft(IC<t - 1>{}); });
      if (hf == 0) issue(t + LA);
    };
    kstep(IC<0>{}); kstep(IC<1>{}); kstep(IC<2>{}); kstep(IC<3>{}); kstep(IC<4>{}); kstep(IC<5>{}); kstep(IC<6>{}); kstep(IC<7>{});
    kstep(IC<8>{}); kstep(IC<9>{}); kstep(IC<10>{}); kstep(IC<11>{}); kstep(IC<12>{}); kstep(IC<13>{}); kstep(IC<14>{}); kstep(IC<15>{});
    if (attempt == STAMP_ATT) STAMP(2);
    f32x4 la[2];
    bf16x8 ones;
#pragma unroll
    for (int j = 0; j < 8; ++j) ones[j] = 0x3f80;
    auto lmm = [&](auto cc) {
      constexpr int c = decltype(cc)::value;
#pragma unroll
      for (int u = 0; u < 4; ++u)
#pragma unroll
        for (int qb = 0; qb < 2; ++qb) la[qb] = MFMA16(ones, __builtin_bit_cast(bf16x8, pst[4 * c + u][qb]), (c == 0 && u == 0) ? (f32x4)(0.f) : la[qb]);
    };
    {
      const unsigned vb = vaddr(0); wbar<4 * (LA - 1)>(); if (hf == 1) issue(16 + LA);
      ring_step<16, NBV, 2>(carry, [&](int f) { return vrd(vb, f); }, [&](int f, bf16x8 a) { vmm(IC<0>{}, f, a); },
                         [&](int r, bf16x8 a) { kmm(IC<15>{}, 16 - RDEF + r, a); }, [&]() { soft(IC<15>{}); lmm(IC<0>{}); });
      if (hf == 0) issue(16 + LA);
    }
    {
      const unsigned vb = vaddr(1); wbar<4 * (LA - 1)>(); if (hf == 1) issue(17 + LA);
      ring_step<16, NBV, 2>(carry, [&](int f) { return vrd(vb, f); }, [&](int f, bf16x8 a) { vmm(IC<1>{}, f, a); },
                         [&](int r, bf16x8 a) { vmm(IC<0>{}, 16 - RDEF + r, a); }, [&]() { lmm(IC<1>{}); });
      if (hf == 0) issue(17 + LA);
    }
    {
      const unsigned vb = vaddr(2); wbar<4 * (LA - 1)>(); if (hf == 1) issue(18 + LA);
      ring_step<16, NBV, 2>(carry, [&](int f) { return vrd(vb, f); }, [&](int f, bf16x8 a) { vmm(IC<2>{}, f, a); },
                         [&](int r, bf16x8 a) { vmm(IC<1>{}, 16 - RDEF + r, a); }, [&]() { lmm(IC<2>{}); lmm(IC<3>{}); });
      const int ln = lane_id();
      const bool bad = !(la[0][0] <= LMAX) || !(la[1][0] <= LMAX);
      const float anybad = __any(bad) ? 1.f : 0.f;
      if (ln < 16) *(LAS f32x4*)(uintptr_t)(lds0 + MISC + w * 256 + ln * 16) = f32x4{mref[0], la[0][0], mref[1], la[1][0]};
      if (ln == 0) *(LAS float*)(uintptr_t)(lds0 + MISC + 2048 + w * 4) = anybad;
      if (hf == 0) issue(18 + LA);
    }
    float fl;
    {
      const unsigned vb = vaddr(3); wbar<4 * (LA - 1)>(); if (hf == 1) issue(19 + LA);
      ring_step<16, NBV, 2>(carry, [&](int f) { return vrd(vb, f); }, [&](int f, bf16x8 a) { vmm(IC<3>{}, f, a); },
                         [&](int r, bf16x8 a) { vmm(IC<2>{}, 16 - RDEF + r, a); },
                         [&]() {
                           const int ln = lane_id();
                           const f32x4 o = *(const LAS f32x4*)(uintptr_t)(lds0 + MISC + (w ^ 4) * 256 + (ln & 15) * 16);
                           const f32x4 f0 = *(const LAS f32x4*)(uintptr_t)(lds0 + MISC + 2048), f1 = *(const LAS f32x4*)(uintptr_t)(lds0 + MISC + 2048 + 16);
                           fl = (f0[0] + f0[1]) + (f0[2] + f0[3]) + (f1[0] + f1[1]) + (f1[2] + f1[3]); OPAQUE(fl);
                           const bool odd = (ln >> 4) & 1;
                           float l0 = la[0][0], l1 = la[1][0], m0 = mref[0], m1 = mref[1], o0_ = o[0], o1_ = o[1], o2_ = o[2], o3_ = o[3];
                           OPAQUE(l0); OPAQUE(l1); OPAQUE(m0); OPAQUE(m1); OPAQUE(o0_); OPAQUE(o1_); OPAQUE(o2_); OPAQUE(o3_);
                           const float ms = odd ? m1 : m0, ls = odd ? l1 : l0, mo = odd ? o2_ : o0_, lo = odd ? o3_ : o1_;
                           const float m = fmaxf(ms, mo), ea = __builtin_amdgcn_exp2f(ms - m), eb = __builtin_amdgcn_exp2f(mo - m);
                           cpk = ea / (ls * ea + lo * eb); OPAQUE(cpk);
                         });
      if (hf == 0) issue(19 + LA);
    }
    if (attempt == 2 || (attempt == 0 && __builtin_amdgcn_readfirstlane(__float_as_uint(fl)) == 0u)) break;
    mref[0] += xmax(tmax[0]); mref[1] += xmax(tmax[1]);
    wbar<0>();
  }

  u32x4 ost[4][2];
  auto comb_write = [&]() {
    MFMA_DRAIN();
    const int ln = lane_id();
    float cc = cpk; OPAQUE(cc);
    const auto cr = __builtin_amdgcn_permlane16_swap(__float_as_uint(cc), __float_as_uint(cc), false, false);
    const float c0 = __uint_as_float(cr[0]), c1 = __uint_as_float(cr[1]);
#pragma unroll
    for (int db = 0; db < 4; ++db) { oa[db][0] *= c0; oa[db][1] *= c1; }
#pragma unroll
    for (int k = 0; k < 2; ++k)
#pragma unroll
      for (int qb = 0; qb < 2; ++qb) *(LAS f32x4*)(uintptr_t)(lds0 + MISC + w * 4096 + (k * 2 + qb) * 1024 + ln * 16) = oa[2 + k][qb];
  };
  f32x4 okeep[2][2];
  auto comb_read = [&](auto jj) {
    constexpr int j = decltype(jj)::value;
    const unsigned pad = lds0 + MISC + (w ^ 4) * 4096 + lane_id() * 16;
#pragma unroll
    for (int qb = 0; qb < 2; ++qb) {
      const f32x4 a = *(const LAS f32x4*)(uintptr_t)(pad + qb * 1024) + okeep[0][qb];
      const f32x4 c = *(const LAS f32x4*)(uintptr_t)(pad + (2 + qb) * 1024) + okeep[1][qb];
      ost[j][qb] = u32x4{cvt2(a[0], a[1]), cvt2(a[2], a[3]), cvt2(c[0], c[1]), cvt2(c[2], c[3])};
      OPAQUE(ost[j][qb]);
    }
  };
  auto vstep = [&](auto nn) {
    constexpr int n = decltype(nn)::value, c = n & 3;
    const unsigned vb = vaddr(n & 3);
    wbar<4 * (LA - 1)>();
    if (hf == 1) issue(n + 16 + LA);
    ring_step<16, NBV, 2>(carry, [&](int f) { return vrd(vb, f); }, [&](int f, bf16x8 a) { vmm(IC<c>{}, f, a); },
                          [&](int r, bf16x8 a) { vmm(IC<(c + 3) & 3>{}, 16 - RDEF + r, a); },
                          [&]() {
                            if constexpr (c == 0) { comb_write(); okeep[0][0] = oa[0][0]; okeep[0][1] = oa[0][1]; okeep[1][0] = oa[1][0]; okeep[1][1] = oa[1][1];
                                                    PIN2(okeep[0][0], okeep[0][1]); PIN2(okeep[1][0], okeep[1][1]); }
                            if constexpr (c == 1) comb_read(IC<(n >> 2) - 1>{});
                          });
    if (hf == 0) issue(n + 16 + LA);
  };
  vstep(IC<4>{}); vstep(IC<5>{}); vstep(IC<6>{}); vstep(IC<7>{}); vstep(IC<8>{}); vstep(IC<9>{});
  vstep(IC<10>{}); vstep(IC<11>{}); vstep(IC<12>{}); vstep(IC<13>{}); vstep(IC<14>{}); vstep(IC<15>{});
  STAMP(3);
#pragma unroll
  for (int r = 0; r < RDEF; ++r) vmm(IC<3>{}, 16 - RDEF + r, carry[r]);
  comb_write(); okeep[0][0] = oa[0][0]; okeep[0][1] = oa[0][1]; okeep[1][0] = oa[1][0]; okeep[1][1] = oa[1][1];
  CBAR();
  comb_read(IC<3>{});
  {
    const int ln = lane_id(), g = ln >> 4;
    const unsigned ow = lds0 + OST + ((rg & 1) * 2 + (rg >> 1)) * 16384 + hf * 2048 + ((g >> 1) * 32 + (ln & 15)) * 16 + (g & 1) * 8;
#pragma unroll
    for (int j = 0; j < 4; ++j) {
      const unsigned oj = ow + ((j + vrot) & 3) * 4096;
#pragma unroll
      for (int qb = 0; qb < 2; ++qb) {
        *(LAS u32x2*)(uintptr_t)(oj + qb * 256) = u32x2{ost[j][qb][0], ost[j][qb][1]};
        *(LAS u32x2*)(uintptr_t)(oj + qb * 256 + 1024) = u32x2{ost[j][qb][2], ost[j][qb][3]};
      }
    }
  }
  wbar<4>();
  unsigned long long* ctl = (unsigned long long*)(ws + (size_t)grp * 2048);
  const __amdgpu_buffer_rsrc_t wsr = __builtin_amdgcn_make_buffer_rsrc(ws, 0, 0x7fffffff, 0x00020000);
  unsigned long long ep;
  if (w >= 4) {
    const int lw = w - 4;
    wo_eighth(2, MISC, 2 * lw); wo_eighth(2, MISC, 2 * lw + 1);
    int base = 0;
    for (int T = 1; T < 14; ++T) {
      if (T < 13) wbar<8>(); else wbar<0>();
      if (T + 2 < 14) { wo_eighth(T + 2, base, 2 * lw); wo_eighth(T + 2, base, 2 * lw + 1); }
      base = base == 0 ? SS : (base == SS ? MISC : 0);
    }
    ep = __hip_atomic_load(ctl, __ATOMIC_RELAXED, __HIP_MEMORY_SCOPE_AGENT);
  } else {
    const int ln = lane_id(), g = ln >> 4, qq = (ln >> 2) & 3, pp = ln & 3, h = ln >> 5;
    const int qh = w & 1, nh = w >> 1;
    bf16x8 of[2][16];
#pragma unroll
    for (int hh = 0; hh < 2; ++hh)
#pragma unroll
      for (int s = 0; s < 16; ++s) of[hh][s] = ldsb128(lds0 + OST + (qh * 2 + hh) * 16384 + s * 1024 + ln * 16);
    ep = __hip_atomic_load(ctl, __ATOMIC_RELAXED, __HIP_MEMORY_SCOPE_AGENT);
    const unsigned abase = lds0 + (8 * h + qq) * 128 + (((4 * nh + 2 * (g & 1) + (pp >> 1)) ^ ((qq & 2) << 1)) << 4) + (pp & 1) * 8;
    bf16x8 fa[8], fbb[8];
#pragma unroll
    for (int a = 0; a < 8; ++a) fbb[a] = (bf16x8)(0);
    f32x16 acc = (f32x16)(0.f), acc2 = (f32x16)(0.f);
    int base = 0;
    auto ostore = [&](int rr) {
      int r = rr + rot7; r = r >= 7 ? r - 7 : r;
      acc += acc2; acc2 = (f32x16)(0.f);
#pragma unroll
      for (int a = 0; a < 2; ++a) {
        u32x4 f = {cvt2(acc[8 * a], acc[8 * a + 1]), cvt2(acc[8 * a + 2], acc[8 * a + 3]), cvt2(acc[8 * a + 4], acc[8 * a + 5]), cvt2(acc[8 * a + 6], acc[8 * a + 7])};
        *(LAS u32x4*)(uintptr_t)(lds0 + OST + (r * 8 + w * 2 + a) * 1024 + ln * 16) = f;
      }
      acc = (f32x16)(0.f);
    };
    auto otile = [&](auto hh_, bool first, int rr) {
      constexpr int hh = decltype(hh_)::value;
      unsigned p = abase + base; OPAQUE(p);
      base = base == 0 ? SS : (base == SS ? MISC : 0);
#pragma unroll
      for (int a = 0; a < 8; ++a) {
        fa[a] = cat8(ldstr(p + a * 2048), ldstr(p + a * 2048 + 512));
        if (a & 1) acc2 = MFMA32(fbb[a], of[hh ^ 1][8 + a], acc2); else acc = MFMA32(fbb[a], of[hh ^ 1][8 + a], acc);
        SBAR();
      }
      if (hh == 0 && !first) ostore(rr - 1);
      SBAR();
#pragma unroll
      for (int a = 0; a < 8; ++a) {
        if (a < 4) {
#pragma unroll
          for (int u = 0; u < 2; ++u) fbb[2 * a + u] = cat8(ldstr(p + (8 + 2 * a + u) * 2048), ldstr(p + (8 + 2 * a + u) * 2048 + 512));
        }
        if (a & 1) acc2 = MFMA32(fa[a], of[hh][a], acc2); else acc = MFMA32(fa[a], of[hh][a], acc);
        SBAR();
      }
    };
    for (int rr = 0; rr < 7; ++rr) {
      if (rr > 0) CBAR();
      otile(IC<0>{}, rr == 0, rr);
      if (rr == 0) wbar<1>(); else CBAR();
      otile(IC<1>{}, false, rr);
    }
#pragma unroll
    for (int a = 0; a < 8; ++a) { if (a & 1) acc2 = MFMA32(fbb[a], of[1][8 + a], acc2); else acc = MFMA32(fbb[a], of[1][8 + a], acc); }
    ostore(6);
  }
  const int lane = lane_id();
  CBAR();
#pragma unroll
  for (int i = 0; i < 7; ++i) {
    const u32x4 f = *(const LAS u32x4*)(uintptr_t)(lds0 + OST + (i * 8 + w) * 1024 + lane * 16);
    const unsigned off = (unsigned)WS_PART + (((unsigned)(grp * 56 + i * 8 + w) * 8 + kvh) << 10) + lane * 16;
    __builtin_amdgcn_raw_buffer_store_b128(f, wsr, off, 0, 16);
  }
  const unsigned long long tag = ep * 0x9E3779B97F4A7C15ull + 0x632BE59BD9B4E019ull;
  STAMP(4);
  wbar<0>();
  STAMP(5);
  if (w == 0 && lane == 0) __hip_atomic_store(ctl + 16 * (kvh + 1), tag, __ATOMIC_RELAXED, __HIP_MEMORY_SCOPE_AGENT);
  if (w < 7) {
    for (unsigned spin = 0; spin < (1u << 22); ++spin) {
      unsigned long long f = tag;
      if (lane < 8) f = __hip_atomic_load(ctl + 16 * (lane + 1), __ATOMIC_RELAXED, __HIP_MEMORY_SCOPE_AGENT);
      if (__all(f == tag)) break;
      __builtin_amdgcn_s_sleep(2);
    }
    __builtin_amdgcn_fence(__ATOMIC_ACQUIRE, "wavefront");
    STAMP(6);
    const int pc = kvh * 7 + w, r = pc >> 3, pw = (pc >> 1) & 3, a = pc & 1;
    const unsigned off = (unsigned)WS_PART + ((unsigned)(grp * 56 + pc) << 13) + lane * 16;
    u32x4 v[8];
#pragma unroll
    for (int s = 0; s < 8; ++s) v[s] = __builtin_amdgcn_raw_buffer_load_b128(wsr, off + s * 1024, 0, 16);
    float acc[8];
#pragma unroll
    for (int e = 0; e < 8; ++e) acc[e] = 0.f;
#pragma unroll
    for (int s = 0; s < 8; ++s)
#pragma unroll
      for (int e = 0; e < 4; ++e) { acc[2 * e] += __uint_as_float(v[s][e] << 16); acc[2 * e + 1] += __uint_as_float(v[s][e] & 0xffff0000u); }
    const int q = 32 * (pw & 1) + (lane & 31), n = 64 * r + 32 * (pw >> 1) + 16 * a + 4 * (lane >> 5);
    char* o = out + ((size_t)(b * SQ + q) * (2 * DM) + br * DM + n) * 2;
    *(u32x2*)o = u32x2{cvt2(acc[0], acc[1]), cvt2(acc[2], acc[3])};
    *(u32x2*)(o + 16) = u32x2{cvt2(acc[4], acc[5]), cvt2(acc[6], acc[7])};
    STAMP(7);
    if (kvh == 0 && w == 0 && lane == 0) __hip_atomic_store(ctl, tag, __ATOMIC_RELAXED, __HIP_MEMORY_SCOPE_AGENT);
  }
}

extern "C" void kernel_launch(void* const* d_in, const int* in_sizes, int n_in, void* d_out, int out_size, void* d_ws, size_t ws_size,
                              hipStream_t stream) {
  (void)in_sizes; (void)n_in; (void)out_size; (void)ws_size;
  (void)hipFuncSetAttribute((const void*)gqa_fused_kernel, hipFuncAttributeMaxDynamicSharedMemorySize, LDS_BYTES);
  gqa_fused_kernel<<<256, 512, LDS_BYTES, stream>>>(
      (const char*)d_in[0], (const char*)d_in[1], (const char*)d_in[2], (const char*)d_in[3], (const char*)d_in[4],
      (const char*)d_in[5], (const char*)d_in[6], (const char*)d_in[7], (const char*)d_in[8], (const char*)d_in[9],
      (char*)d_out, (char*)d_ws);
}
```

```cpp
#include <hip/hip_runtime.h>
#include <hip/hip_bf16.h>
#include <stdint.h>
#include <stdio.h>
#include <type_traits>

typedef __attribute__((ext_vector_type(8))) short bf16x8;
typedef __attribute__((ext_vector_type(4))) short s16x4;
typedef __attribute__((ext_vector_type(4))) float f32x4;
typedef __attribute__((ext_vector_type(16))) float f32x16;
typedef __attribute__((ext_vector_type(2))) float f32x2;
typedef __attribute__((ext_vector_type(2))) __bf16 bf16x2_t;
typedef __attribute__((ext_vector_type(4))) unsigned u32x4;
typedef __attribute__((ext_vector_type(2))) unsigned u32x2;
#define LAS __attribute__((address_space(3)))
typedef LAS char* lds_ptr;

constexpr int SQ = 64, DM = 448, HKV = 8, SK = 1024, DH = 256;
constexpr int SS = 32768;
constexpr int MISC = 4 * SS;
constexpr int LDS_BYTES = 5 * SS;
constexpr int XIMG = 3 * SS;
constexpr int QST = 3 * SS;
constexpr float CEXP = 0.0625f * 1.4426950408889634f;
#define LMAX 1.0e18f
#define STAMP_ATT 0
#define NBK 5
#define NBV 6
#define NBW 4
constexpr int RDEF = 2;

constexpr size_t WS_PART = 65536;

#define MFMA16(a, b, c) __builtin_amdgcn_mfma_f32_16x16x32_bf16((a), (b), (c), 0, 0, 0)
#define MFMA32(a, b, c) __builtin_amdgcn_mfma_f32_32x32x16_bf16((a), (b), (c), 0, 0, 0)
#define CBAR() asm volatile("s_waitcnt lgkmcnt(0)\n\ts_barrier" ::: "memory")
#define SBAR() __builtin_amdgcn_sched_barrier(0)
#define OPAQUE(x) asm volatile("" : "+v"(x))
#define PIN2(x, y) asm volatile("" : "+v"(x), "+v"(y))
#define SOPAQUE(x) asm volatile("" : "+s"(x))
#define DI __device__ __forceinline__
template <int N> DI void wbar() { asm volatile("s_waitcnt vmcnt(%0) lgkmcnt(0)\n\ts_barrier" ::"i"(N) : "memory"); }
DI void bar() { asm volatile("s_barrier" ::: "memory"); }
template <int N> using IC = std::integral_constant<int, N>;

DI unsigned cvt2(float a, float b) { f32x2 v = {a, b}; return __builtin_bit_cast(unsigned, __builtin_convertvector(v, bf16x2_t)); }
DI bf16x8 ldsb128(unsigned a) { return *(const LAS bf16x8*)(uintptr_t)a; }
DI s16x4 ldstr(unsigned a) { return __builtin_amdgcn_ds_read_tr16_b64_v4i16((LAS s16x4*)(uintptr_t)a); }
DI bf16x8 cat8(s16x4 a, s16x4 b) { return __builtin_shufflevector(a, b, 0, 1, 2, 3, 4, 5, 6, 7); }
DI int lane_id() { int l; asm volatile("v_mbcnt_lo_u32_b32 %0, -1, 0\n\tv_mbcnt_hi_u32_b32 %0, -1, %0" : "=v"(l)); return l; }
DI float xsum(float v) {
  { auto r = __builtin_amdgcn_permlane16_swap(__float_as_uint(v), __float_as_uint(v), false, false); v = __uint_as_float(r[0]) + __uint_as_float(r[1]); }
  { auto r = __builtin_amdgcn_permlane32_swap(__float_as_uint(v), __float_as_uint(v), false, false); v = __uint_as_float(r[0]) + __uint_as_float(r[1]); }
  return v;
}
DI float xmax(float v) {
  { auto r = __builtin_amdgcn_permlane16_swap(__float_as_uint(v), __float_as_uint(v), false, false); v = fmaxf(__uint_as_float(r[0]), __uint_as_float(r[1])); }
  { auto r = __builtin_amdgcn_permlane32_swap(__float_as_uint(v), __float_as_uint(v), false, false); v = fmaxf(__uint_as_float(r[0]), __uint_as_float(r[1])); }
  return v;
}

DI void mm2(f32x4& c0, f32x4& c1, bf16x8 a, bf16x8 b0, bf16x8 b1) {
  asm volatile("v_mfma_f32_16x16x32_bf16 %0, %2, %3, %0\n\tv_mfma_f32_16x16x32_bf16 %1, %2, %4, %1" : "+v"(c0), "+v"(c1) : "v"(a), "v"(b0), "v"(b1));
}
DI void mm2z(f32x4& c0, f32x4& c1, bf16x8 a, bf16x8 b0, bf16x8 b1) {
  asm volatile("v_mfma_f32_16x16x32_bf16 %0, %2, %3, 0\n\tv_mfma_f32_16x16x32_bf16 %1, %2, %4, 0" : "=&v"(c0), "=&v"(c1) : "v"(a), "v"(b0), "v"(b1));
}
DI void mm2c(f32x4& c0, f32x4& c1, bf16x8 a, bf16x8 b0, bf16x8 b1, f32x4 i0, f32x4 i1) {
  asm volatile("v_mfma_f32_16x16x32_bf16 %0, %2, %3, %5\n\tv_mfma_f32_16x16x32_bf16 %1, %2, %4, %6" : "=&v"(c0), "=&v"(c1) : "v"(a), "v"(b0), "v"(b1), "v"(i0), "v"(i1));
}
#define MFMA_DRAIN() do { asm volatile("s_nop 7\n\ts_nop 1" ::: "memory"); SBAR(); } while (0)

DI void dma4(unsigned lds, const void* sbase, unsigned v0, unsigned v1, unsigned v2, unsigned v3) {
  unsigned keep;
  asm volatile("s_mov_b32 %0, m0\n\ts_mov_b32 m0, %1\n\ts_nop 3\n\t"
               "global_load_lds_dwordx4 %2, %6\n\tglobal_load_lds_dwordx4 %3, %6 offset:1024\n\t"
               "global_load_lds_dwordx4 %4, %6 offset:2048\n\tglobal_load_lds_dwordx4 %5, %6 offset:3072\n\ts_mov_b32 m0, %0"
               : "=&s"(keep) : "s"(lds), "v"(v0), "v"(v1), "v"(v2), "v"(v3), "s"(sbase) : "memory");
}
DI void dma2(unsigned lds, const void* sbase, unsigned v0, unsigned v1) {
  unsigned keep;
  asm volatile("s_mov_b32 %0, m0\n\ts_mov_b32 m0, %1\n\ts_nop 3\n\t"
               "global_load_lds_dwordx4 %2, %4\n\tglobal_load_lds_dwordx4 %3, %4 offset:1024\n\ts_mov_b32 m0, %0"
               : "=&s"(keep) : "s"(lds), "v"(v0), "v"(v1), "s"(sbase) : "memory");
}
DI void dma4s(unsigned lds, const char* s, size_t gstride, unsigned v) {
  unsigned keep;
  asm volatile("s_mov_b32 %0, m0\n\ts_mov_b32 m0, %1\n\ts_nop 3\n\tglobal_load_lds_dwordx4 %2, %3 nt\n\t"
               "s_add_u32 m0, m0, 0x2000\n\ts_nop 0\n\tglobal_load_lds_dwordx4 %2, %4 nt\n\t"
               "s_add_u32 m0, m0, 0x2000\n\ts_nop 0\n\tglobal_load_lds_dwordx4 %2, %5 nt\n\t"
               "s_add_u32 m0, m0, 0x2000\n\ts_nop 0\n\tglobal_load_lds_dwordx4 %2, %6 nt\n\ts_mov_b32 m0, %0"
               : "=&s"(keep) : "s"(lds), "v"(v), "s"(s), "s"(s + gstride), "s"(s + 2 * gstride), "s"(s + 3 * gstride) : "memory", "scc");
}

DI void dma4s_keep(unsigned lds, const char* s, size_t gstride, unsigned v) {
  unsigned keep;
  asm volatile("s_mov_b32 %0, m0\n\ts_mov_b32 m0, %1\n\ts_nop 3\n\tglobal_load_lds_dwordx4 %2, %3\n\t"
               "s_add_u32 m0, m0, 0x400\n\ts_nop 0\n\tglobal_load_lds_dwordx4 %2, %4\n\t"
               "s_add_u32 m0, m0, 0x400\n\ts_nop 0\n\tglobal_load_lds_dwordx4 %2, %5\n\t"
               "s_add_u32 m0, m0, 0x400\n\ts_nop 0\n\tglobal_load_lds_dwordx4 %2, %6\n\ts_mov_b32 m0, %0"
               : "=&s"(keep) : "s"(lds), "v"(v), "s"(s), "s"(s + gstride), "s"(s + 2 * gstride), "s"(s + 3 * gstride) : "memory", "scc");
}

template <int NF, int NB, int NI, class RD, class MM, class PREV, class MID, class HOOK>
DI void ring_step(bf16x8 (&carry)[RDEF], RD rd, MM mm, PREV prev, MID mid, HOOK hook) {
  static_assert(NI + RDEF <= NB, "");
  bf16x8 fb[NB];
#pragma unroll
  for (int f = 0; f < NI; ++f) fb[f] = rd(f);
  SBAR();
#pragma unroll
  for (int r = 0; r < RDEF; ++r) { prev(r, carry[r]); SBAR(); fb[NI + r] = rd(NI + r); SBAR(); }
  mid();
  SBAR();
#pragma unroll
  for (int f = NI + RDEF; f < NB; ++f) fb[f] = rd(f);
  SBAR();
#pragma unroll
  for (int f = 0; f < NF - RDEF; ++f) {
    mm(f, fb[f % NB]); SBAR();
    if (f + NB < NF) { fb[f % NB] = rd(f + NB); SBAR(); }
    hook(f); SBAR();
  }
#pragma unroll
  for (int r = 0; r < RDEF; ++r) carry[r] = fb[(NF - RDEF + r) % NB];
}

#define TS(k) do {} while (0)
#define STAMP(i) do {} while (0)

__global__ __launch_bounds__(512, 2) void gqa_fused_kernel(
    const char* __restrict__ xA, const char* __restrict__ kA, const char* __restrict__ vA, const char* __restrict__ wqA, const char* __restrict__ woA,
    const char* __restrict__ xB, const char* __restrict__ kB, const char* __restrict__ vB, const char* __restrict__ wqB, const char* __restrict__ woB,
    char* __restrict__ out, char* __restrict__ ws) {
  extern __shared__ __attribute__((aligned(16))) char smem[];
  const unsigned lds0 = (unsigned)(uintptr_t)(lds_ptr)smem;
  int w = __builtin_amdgcn_readfirstlane(threadIdx.x >> 6);
  const int id = blockIdx.x, br = (id >> 2) & 1, kvh = 2 * (id & 3) + ((id >> 3) & 1), b = id >> 4, grp = br * 16 + b;
  int rot7 = b % 7;
  int rg = w & 3, hf = w >> 2;
  STAMP(0);
  const char* kg = (br ? kB : kA) + (size_t)(b * HKV + kvh) * (SK * DH * 2);
  const char* vg = (br ? vB : vA) + (size_t)(b * HKV + kvh) * (SK * DH * 2);
  const char* wog = (br ? woB : woA) + (size_t)kvh * 512 * (DM * 2);

  auto k_eighth = [&](int t, int e) {
    const int ln = lane_id(), hi = ln >> 5;
    const char* kb = kg; SOPAQUE(kb);
    dma4s(lds0 + (t & 3) * SS + e * 1024, kb + (size_t)t * SS + e * 1024, 8192, hi * 512 + (((ln & 31) ^ (2 * e + hi)) << 4));
  };

  int vrot = (id >> 3) & 3;
  auto issue_vw = [&](int p) {
    const unsigned dst = lds0 + (p & 3) * SS + w * 1024;
    const int ln = lane_id();
    const bool isv = p < 20 || (p < 44 && !(p & 1));
    if (isv) {
      const int n = p < 20 ? p - 16 : 4 + ((p - 20) >> 1);
      const int r8 = ln >> 3;
      const char* vb = vg; SOPAQUE(vb);
      dma4s(dst, vb + (size_t)((n & 3) * 256 + 8 * w) * 512 + (((n >> 2) + vrot) & 3) * 128, 64 * 512, r8 * 512 + (((ln & 7) ^ (r8 & 6)) << 4));
    } else {
      const int k = p < 44 ? (p - 20) >> 1 : p - 32, r8 = ln >> 3;
      const char* wb = wog; SOPAQUE(wb);
      dma4s_keep(lds0 + (p & 3) * SS + w * 4096, wb + (size_t)(((k >> 1) & 1) * 256 + (((k >> 2) + vrot) & 3) * 64 + (k & 1) * 32) * 896 + (w < 7 ? w : 6) * 128,
                 8 * 896, r8 * 896 + (((ln & 7) ^ (r8 & 6)) << 4));
    }
  };
  auto issue = [&](int p) { if (p < 16) k_eighth(p, w); else issue_vw(p); };

  u32x4 pst[16][2];
  f32x4 oa[4][2];
  bf16x8 carry[RDEF];
  float mref[2] = {-INFINITY, -INFINITY};
  float cpk;

  auto vaddr = [&](int slot) {
    const int ln = lane_id(), rl = ((ln >> 4) << 2) | ((ln >> 2) & 3);
    unsigned a = lds0 + slot * SS + hf * 4096 + rl * 128 + ((((ln >> 1) & 1) ^ (rl & 6) ^ (hf << 2)) << 4) + (ln & 1) * 8;
    OPAQUE(a);
    return a;
  };
  auto vu = [](int c, int f) { return c == 0 ? ((f >> 1) & 3) : (f >> 2); };
  auto vd = [](int c, int f) { return c == 0 ? ((f & 1) | ((f & 8) ? 0 : 2)) : (f & 3); };
  auto vrd = [&](auto cc, unsigned vb, int f) {
    constexpr int c = decltype(cc)::value;
    const unsigned a = (vb ^ (unsigned)(vd(c, f) << 5)) + vu(c, f) * 8192; return cat8(ldstr(a), ldstr(a + 2048));
  };
  auto vmm = [&](auto cc, int f, bf16x8 a) {
    constexpr int c = decltype(cc)::value;
    const int u = vu(c, f), db = vd(c, f);
    const bf16x8 b0 = __builtin_bit_cast(bf16x8, pst[4 * c + u][0]), b1 = __builtin_bit_cast(bf16x8, pst[4 * c + u][1]);
    if (c == 0 && u == 0) mm2z(oa[db][0], oa[db][1], a, b0, b1); else mm2(oa[db][0], oa[db][1], a, b0, b1);
  };
  auto nohook = [](int) {};
  auto nop = []() {};

  for (int attempt = 0;; ++attempt) {
  SOPAQUE(w); SOPAQUE(rot7); SOPAQUE(vrot); rg = w & 3; hf = w >> 2;
  if (w >= 4) {
    const int lw = w - 4, lane = lane_id();
    const char* xg = (br ? xB : xA) + (size_t)b * (SQ * DM * 2); SOPAQUE(xg);
    const char* wqg = (br ? wqB : wqA) + (size_t)kvh * 1024; SOPAQUE(wqg);
    unsigned wofs[4];
#pragma unroll
    for (int j = 0; j < 4; ++j) wofs[j] = j * (8192 - 1024) + ((lane ^ (j << 2)) << 4);
    int wsl = 1;
    auto wtile = [&](int i) {
      int G = (i >> 1) + rot7; G = G >= 7 ? G - 7 : G;
      const char* src = wqg + (size_t)(G * 64 + (i & 1) * 32 + 8 * lw) * 8192;
      const unsigned dst = lds0 + wsl * SS + lw * 8192;
      dma4(dst, src, wofs[0], wofs[1], wofs[2], wofs[3]);
      dma4(dst + 4096, src + 4 * 8192, wofs[0], wofs[1], wofs[2], wofs[3]);
      wsl = wsl == 2 ? 0 : wsl + 1;
    };
    const int xr = 16 * lw + (lane >> 3), xc = lane & 7;
    const unsigned x0 = xr * 896 + ((xc ^ ((xr >> 1) & 7)) << 4), x1 = (xr + 8) * 896 + ((xc ^ (((xr + 8) >> 1) & 7)) << 4) - 1024;
    auto xtile = [&](int Gi) { int G = Gi + rot7; G = G >= 7 ? G - 7 : G; dma2(lds0 + XIMG + Gi * 8192 + lw * 2048, xg + G * 128, x0, x1); };
    xtile(0); wtile(0); wtile(1);
    for (int i = 0; i < 14; ++i) {
      if (i == 1) wbar<20>(); else wbar<8>();
      if (i == 0) for (int Gi = 1; Gi < 7; ++Gi) xtile(Gi);
      if (i + 2 < 14) wtile(i + 2); else { k_eighth(i - 12, 2 * lw); k_eighth(i - 12, 2 * lw + 1); }
    }
  } else {
    const int lane = lane_id(), g = lane >> 4, qq = (lane >> 2) & 3, pp = lane & 3;
    const int mh = w & 1, hh = w >> 1, h = lane >> 5, m = lane & 31;
    f32x16 qa[8];
#pragma unroll
    for (int a = 0; a < 8; ++a) qa[a] = (f32x16)(0.f);
    const unsigned wbase = lds0 + (8 * h + qq) * 1024 + hh * 512 + (((2 * (g & 1) + (pp >> 1)) ^ (qq << 2)) << 4) + (pp & 1) * 8;
    const unsigned xbase = lds0 + XIMG + (32 * mh + m) * 128 + ((h ^ ((m >> 1) & 7)) << 4);
    bf16x8 wfA[8], wfB[8], xa, xb, xo, xc = (bf16x8)(0), xd = (bf16x8)(0);
#pragma unroll
    for (int a = 0; a < 8; ++a) wfB[a] = (bf16x8)(0);
    xo = (bf16x8)(0);
    int sl = SS;
    for (int i = 0; i < 14; ++i) {
      CBAR();
      unsigned p = wbase + sl; OPAQUE(p);
      sl = sl == 2 * SS ? 0 : sl + SS;
      const unsigned xp = (xbase + (i >> 1) * 8192) ^ (unsigned)((i & 1) << 6);
      if (i < 13) { xa = ldsb128(xp); xb = ldsb128(xp ^ 32); } else { xa = xc; xb = xd; }
      if (i == 12) { xc = ldsb128(xp ^ 64); xd = ldsb128(xp ^ 96); }
      SBAR();
#pragma unroll
      for (int a = 0; a < 8; ++a) {
        const unsigned q = (p ^ (unsigned)((a & 3) << 6)) + (a >> 2) * 256; wfA[a] = cat8(ldstr(q), ldstr(q + 4096));
        qa[a] = MFMA32(wfB[a], xo, qa[a]);
        SBAR();
      }
#pragma unroll
      for (int a = 0; a < 8; ++a) {
        if (a < 4) {
#pragma unroll
          for (int u = 0; u < 2; ++u) { const int a2 = 2 * a + u; const unsigned q = (p ^ (unsigned)((a2 & 3) << 6)) + (a2 >> 2) * 256; wfB[a2] = cat8(ldstr(q + 16384), ldstr(q + 16384 + 4096)); }
        }
        qa[a] = MFMA32(wfA[a], xa, qa[a]);
        SBAR();
      }
      xo = xb;
    }
    const unsigned qw = lds0 + QST + (hh * 2 + mh) * 16384 + (m >> 4) * 8192 + (m & 15) * 16 + h * 8;
#pragma unroll
    for (int nb = 0; nb < 8; ++nb) {
      qa[nb] = MFMA32(wfB[nb], xo, qa[nb]);
#pragma unroll
      for (int a = 0; a < 4; ++a)
        *(LAS u32x2*)(uintptr_t)(qw + nb * 1024 + a * 256) =
            u32x2{cvt2(qa[nb][4 * a] * CEXP, qa[nb][4 * a + 1] * CEXP), cvt2(qa[nb][4 * a + 2] * CEXP, qa[nb][4 * a + 3] * CEXP)};
    }
  }
  CBAR();
  bf16x8 qf[2][8];
  {
    const unsigned qr = lds0 + QST + rg * 16384 + lane_id() * 16;
#pragma unroll
    for (int qb = 0; qb < 2; ++qb)
#pragma unroll
      for (int s = 0; s < 8; ++s) qf[qb][s] = ldsb128(qr + (qb * 8 + s) * 1024);
  }
  if (attempt == STAMP_ATT) STAMP(1);

    f32x4 sa[2][2];
    f32x4 nm[2];
    float tmax[2] = {0.f, 0.f}, lsum[2] = {0.f, 0.f};
    auto kmm = [&](auto tt, int f, bf16x8 a) {
      constexpr int t = decltype(tt)::value;
      if (f >= 2) mm2(sa[f & 1][0], sa[f & 1][1], a, qf[0][f >> 1], qf[1][f >> 1]);
      else if (t == 0) mm2z(sa[f][0], sa[f][1], a, qf[0][0], qf[1][0]);
      else mm2c(sa[f][0], sa[f][1], a, qf[0][0], qf[1][0], nm[0], nm[1]);
    };
    auto smax = [&](int qb) {
      return fmaxf(fmaxf(fmaxf(sa[0][qb][0], sa[0][qb][1]), fmaxf(sa[0][qb][2], sa[0][qb][3])), fmaxf(fmaxf(sa[1][qb][0], sa[1][qb][1]), fmaxf(sa[1][qb][2], sa[1][qb][3])));
    };
    auto soft = [&](auto tt) {
      constexpr int t = decltype(tt)::value;
      MFMA_DRAIN();
#pragma unroll
      for (int qb = 0; qb < 2; ++qb) {
        if (t == 0) {
          mref[qb] = fmaxf(mref[qb], xmax(smax(qb)));
          nm[qb] = (f32x4)(-mref[qb]); OPAQUE(nm[qb]);
          sa[0][qb] += nm[qb]; sa[1][qb] += nm[qb];
        } else if (attempt == 1) tmax[qb] = fmaxf(tmax[qb], smax(qb));
        float e[8];
#pragma unroll
        for (int r = 0; r < 8; ++r) e[r] = __builtin_amdgcn_exp2f(sa[r >> 2][qb][r & 3]);
        lsum[qb] += ((e[0] + e[1]) + (e[2] + e[3])) + ((e[4] + e[5]) + (e[6] + e[7])); OPAQUE(lsum[qb]);
        pst[t][qb] = u32x4{cvt2(e[0], e[1]), cvt2(e[2], e[3]), cvt2(e[4], e[5]), cvt2(e[6], e[7])};
        OPAQUE(pst[t][qb]);
      }
    };
    auto kstep = [&](auto tt) {
      constexpr int t = decltype(tt)::value;
      const int ln = lane_id();
      unsigned kb0 = lds0 + (t & 3) * SS + hf * 16384 + (ln & 15) * 512 + (((ln >> 4) ^ (ln & 15)) << 4); OPAQUE(kb0);
      if (t == 0) wbar<8>(); else wbar<4>();
      if (hf == 1) issue(t + 2);
      ring_step<16, NBK, 2>(carry,
          [&](int f) { return ldsb128((kb0 ^ (unsigned)(((f >> 1) & 3) << 6)) + (f >> 3) * 256 + (f & 1) * 8192); },
          [&](int f, bf16x8 a) { kmm(IC<t>{}, f, a); },
          [&](int r, bf16x8 a) { if constexpr (t > 0) kmm(IC<t - 1>{}, 16 - RDEF + r, a); },
          [&]() { if constexpr (t > 0) soft(IC<t - 1>{}); }, nohook);
      if (hf == 0) issue(t + 2);
    };
    kstep(IC<0>{}); kstep(IC<1>{}); kstep(IC<2>{}); kstep(IC<3>{}); kstep(IC<4>{}); kstep(IC<5>{}); kstep(IC<6>{}); kstep(IC<7>{});
    kstep(IC<8>{}); kstep(IC<9>{}); kstep(IC<10>{}); kstep(IC<11>{}); kstep(IC<12>{}); kstep(IC<13>{}); kstep(IC<14>{}); kstep(IC<15>{});
    if (attempt == STAMP_ATT) STAMP(2);
    {
      const unsigned vb = vaddr(0); wbar<4>(); if (hf == 1) { issue_vw(18); issue_vw(19); }
      ring_step<16, NBV, 2>(carry, [&](int f) { return vrd(IC<0>{}, vb, f); }, [&](int f, bf16x8 a) { vmm(IC<0>{}, f, a); },
                            [&](int r, bf16x8 a) { kmm(IC<15>{}, 16 - RDEF + r, a); },
                            [&]() {
                              soft(IC<15>{});
                              const int ln = lane_id();
                              lsum[0] = xsum(lsum[0]); lsum[1] = xsum(lsum[1]);
                              const bool bad = !(lsum[0] <= LMAX) || !(lsum[1] <= LMAX);
                              const float anybad = __any(bad) ? 1.f : 0.f;
                              if (ln < 16) *(LAS f32x4*)(uintptr_t)(lds0 + MISC + w * 256 + ln * 16) = f32x4{mref[0], lsum[0], mref[1], lsum[1]};
                              if (ln == 0) *(LAS float*)(uintptr_t)(lds0 + MISC + 2048 + w * 4) = anybad;
                            }, nohook);
      if (hf == 0) { issue_vw(18); issue_vw(19); }
    }
    float fl;
    {
      const unsigned vb = vaddr(1); wbar<8>(); if (hf == 1) issue_vw(20);
      ring_step<16, NBV, 2>(carry, [&](int f) { return vrd(IC<1>{}, vb, f); }, [&](int f, bf16x8 a) { vmm(IC<1>{}, f, a); },
                         [&](int r, bf16x8 a) { vmm(IC<0>{}, 16 - RDEF + r, a); },
                         [&]() {
                           const int ln = lane_id();
                           const f32x4 o = *(const LAS f32x4*)(uintptr_t)(lds0 + MISC + (w ^ 4) * 256 + (ln & 15) * 16);
                           const f32x4 f0 = *(const LAS f32x4*)(uintptr_t)(lds0 + MISC + 2048), f1 = *(const LAS f32x4*)(uintptr_t)(lds0 + MISC + 2048 + 16);
                           fl = (f0[0] + f0[1]) + (f0[2] + f0[3]) + (f1[0] + f1[1]) + (f1[2] + f1[3]); OPAQUE(fl);
                           const bool odd = (ln >> 4) & 1;
                           float l0 = lsum[0], l1 = lsum[1], m0 = mref[0], m1 = mref[1], o0_ = o[0], o1_ = o[1], o2_ = o[2], o3_ = o[3];
                           OPAQUE(l0); OPAQUE(l1); OPAQUE(m0); OPAQUE(m1); OPAQUE(o0_); OPAQUE(o1_); OPAQUE(o2_); OPAQUE(o3_);
                           const float ms = odd ? m1 : m0, ls = odd ? l1 : l0, mo = odd ? o2_ : o0_, lo = odd ? o3_ : o1_;
                           const float m = fmaxf(ms, mo), ea = __builtin_amdgcn_exp2f(ms - m), eb = __builtin_amdgcn_exp2f(mo - m);
                           cpk = ea / (ls * ea + lo * eb); OPAQUE(cpk);
                         }, nohook);
      if (hf == 0) issue_vw(20);
    }
    if (attempt == 2 || (attempt == 0 && __builtin_amdgcn_readfirstlane(__float_as_uint(fl)) == 0u)) break;
    mref[0] += xmax(tmax[0]); mref[1] += xmax(tmax[1]);
    wbar<0>();
  }
  {
    const unsigned vb = vaddr(2); wbar<8>(); if (hf == 1) issue_vw(21);
    ring_step<16, NBV, 2>(carry, [&](int f) { return vrd(IC<2>{}, vb, f); }, [&](int f, bf16x8 a) { vmm(IC<2>{}, f, a); },
                          [&](int r, bf16x8 a) { vmm(IC<1>{}, 16 - RDEF + r, a); }, nop, nohook);
    if (hf == 0) issue_vw(21);
  }
  {
    const unsigned vb = vaddr(3); wbar<8>(); if (hf == 1) issue_vw(22);
    ring_step<16, NBV, 2>(carry, [&](int f) { return vrd(IC<3>{}, vb, f); }, [&](int f, bf16x8 a) { vmm(IC<3>{}, f, a); },
                          [&](int r, bf16x8 a) { vmm(IC<2>{}, 16 - RDEF + r, a); }, nop, nohook);
  }

  const int qh = w >> 2, nq = w & 3;
  f32x4 pa[7][2];
#pragma unroll
  for (int i = 0; i < 7; ++i) { pa[i][0] = (f32x4)(0.f); pa[i][1] = (f32x4)(0.f); }
  bf16x8 o0 = (bf16x8)(0), o1 = (bf16x8)(0);
  auto oload = [&](int idx) {
    const unsigned oad = lds0 + MISC + ((((idx >> 1) * 2 + qh) + 4 * ((idx & 1) ^ 1)) * 4096) + lane_id() * 16;
    o0 = ldsb128(oad); o1 = ldsb128(oad + 1024);
  };
  auto wmm = [&](int i, bf16x8 a) { mm2(pa[i][0], pa[i][1], a, o0, o1); };
  float c0, c1;
  auto comb_write = [&](int k) {
    if (k == 0) {
      float cc = cpk; OPAQUE(cc);
      const auto cr = __builtin_amdgcn_permlane16_swap(__float_as_uint(cc), __float_as_uint(cc), false, false);
      c0 = __uint_as_float(cr[0]); c1 = __uint_as_float(cr[1]);
    }
    if (k < 2) {
      const unsigned ad = lds0 + MISC + w * 4096 + k * 2048 + lane_id() * 16;
      *(LAS f32x4*)(uintptr_t)(ad) = oa[2 + k][0] * c0; *(LAS f32x4*)(uintptr_t)(ad + 1024) = oa[2 + k][1] * c1;
    } else if (k < 4) { oa[k - 2][0] *= c0; oa[k - 2][1] *= c1; PIN2(oa[k - 2][0], oa[k - 2][1]); }
  };
  f32x4 ct0, ct1; u32x4 cf0;
  auto comb_rd = [&](int qb) {
    const unsigned pad = lds0 + MISC + (w ^ 4) * 4096 + qb * 1024 + lane_id() * 16;
    ct0 = *(const LAS f32x4*)(uintptr_t)(pad); ct1 = *(const LAS f32x4*)(uintptr_t)(pad + 2048);
  };
  auto comb_fin = [&](int qb) {
    const f32x4 a = ct0 + oa[0][qb], c = ct1 + oa[1][qb];
    *(LAS u32x4*)(uintptr_t)(lds0 + MISC + (w ^ 4) * 4096 + qb * 1024 + lane_id() * 16) = u32x4{cvt2(a[0], a[1]), cvt2(a[2], a[3]), cvt2(c[0], c[1]), cvt2(c[2], c[3])};
  };
  auto vstep = [&](int slot, int pn, auto cc) {
    constexpr int c = decltype(cc)::value;
    const unsigned vb = vaddr(slot);
    wbar<8>(); TS(c == 0 ? 2 : 1);
    if (hf == 1) issue_vw(pn);
    ring_step<16, (c == 0 ? NBV - 1 : NBV), 2>(carry, [&](int f) { return vrd(cc, vb, f); }, [&](int f, bf16x8 a) { vmm(cc, f, a); },
                          [&](int r, bf16x8 a) { wmm(7 - RDEF + r, a); }, [&]() { if (c == 0) comb_rd(0); },
                          [&](int f) { if (c == 0 && f == 2) { comb_fin(0); comb_rd(1); } if (c == 0 && f == 6) comb_fin(1); });
  };
  auto wstep = [&](int slot, auto pre, auto prev, auto mid, auto hook) {
    const int ln = lane_id(), rl = ((ln >> 4) << 2) | ((ln >> 2) & 3);
    unsigned wb = lds0 + slot * SS + rl * 128 + (((2 * nq + ((ln >> 1) & 1)) ^ (rl & 6)) << 4) + (ln & 1) * 8;
    OPAQUE(wb);
    pre();
    ring_step<7, NBW, NBW - RDEF>(carry, [&](int i) { return cat8(ldstr(wb + i * 4096), ldstr(wb + i * 4096 + 2048)); }, wmm, prev, mid, hook);
  };
#pragma unroll
  for (int r = 0; r < RDEF; ++r) { vmm(IC<3>{}, 16 - RDEF + r, carry[r]); carry[r] = (bf16x8)(0); }
  if (hf == 0) issue_vw(22);
  MFMA_DRAIN();
  comb_write(0); comb_write(1); comb_write(2); comb_write(3);
#pragma unroll 1
  for (int p = 20; p < 44; p += 8) {
    vstep(0, p + 3, IC<0>{}); if (hf == 0) issue_vw(p + 3);
    wstep(1, [&]() { wbar<8>(); TS(3); if (hf == 1) issue_vw(p + 4); oload(0); }, [&](int r, bf16x8 a) { vmm(IC<0>{}, 16 - RDEF + r, a); }, nop, nohook); if (hf == 0) issue_vw(p + 4);
    vstep(2, p + 5, IC<1>{}); if (hf == 0) issue_vw(p + 5);
    wstep(3, [&]() { wbar<8>(); TS(0); if (hf == 1) issue_vw(p + 6); oload(1); }, [&](int r, bf16x8 a) { vmm(IC<1>{}, 16 - RDEF + r, a); }, nop, nohook); if (hf == 0) issue_vw(p + 6);
    vstep(0, p + 7, IC<2>{}); if (hf == 0) issue_vw(p + 7);
    wstep(1, [&]() { wbar<8>(); TS(0); if (hf == 1) issue_vw(p + 8); oload(2); }, [&](int r, bf16x8 a) { vmm(IC<2>{}, 16 - RDEF + r, a); }, nop, nohook); if (hf == 0) issue_vw(p + 8);
    vstep(2, p + 9, IC<3>{}); oload(3); if (hf == 0) issue_vw(p + 9);
    wstep(3, [&]() { wbar<8>(); TS(0); if (hf == 1) issue_vw(p + 10); }, [&](int r, bf16x8 a) { vmm(IC<3>{}, 16 - RDEF + r, a); }, nop,
          [&](int f) { if (f >= 1) comb_write(f - 1); }); if (hf == 0) issue_vw(p + 10);
  }
  STAMP(3);
  CBAR();
  comb_rd(0);
#pragma unroll
  for (int r = 0; r < RDEF; ++r) wmm(7 - RDEF + r, carry[r]);
  comb_fin(0); comb_rd(1); issue_vw(47); comb_fin(1);
  auto wprev = [&](int r, bf16x8 a) { wmm(7 - RDEF + r, a); };
  unsigned long long* ctl = (unsigned long long*)(ws + (size_t)grp * 2048);
  unsigned long long ep;
  wstep(0, [&]() { wbar<8>(); oload(0); }, [&](int, bf16x8) {}, nop, nohook);
  wstep(1, nop, wprev, [&]() { oload(1); }, nohook);
  wstep(2, [&]() { wbar<0>(); ep = __hip_atomic_load(ctl, __ATOMIC_RELAXED, __HIP_MEMORY_SCOPE_AGENT); }, wprev, [&]() { oload(2); }, nohook);
  wstep(3, nop, wprev, [&]() { oload(3); }, nohook);
#pragma unroll
  for (int r = 0; r < RDEF; ++r) wmm(7 - RDEF + r, carry[r]);
  MFMA_DRAIN();
  const int lane = lane_id(), g = lane >> 4, li = lane & 15;
  const __amdgpu_buffer_rsrc_t wsr = __builtin_amdgcn_make_buffer_rsrc(ws, 0, 0x7fffffff, 0x00020000);
#pragma unroll
  for (int i = 0; i < 7; ++i) {
    u32x4 f = {cvt2(pa[i][0][0], pa[i][0][1]), cvt2(pa[i][0][2], pa[i][0][3]), cvt2(pa[i][1][0], pa[i][1][1]), cvt2(pa[i][1][2], pa[i][1][3])};
    const unsigned off = (unsigned)WS_PART + (((unsigned)(grp * 56 + w * 7 + i) * 8 + kvh) << 10) + lane * 16;
    __builtin_amdgcn_raw_buffer_store_b128(f, wsr, off, 0, 16);
  }
  const unsigned long long tag = ep * 0x9E3779B97F4A7C15ull + 0x632BE59BD9B4E019ull;
  STAMP(4);
  wbar<0>();
  STAMP(5);
  if (w == 0 && lane == 0) __hip_atomic_store(ctl + 16 * (kvh + 1), tag, __ATOMIC_RELAXED, __HIP_MEMORY_SCOPE_AGENT);
  if (w < 7) {
    for (unsigned spin = 0; spin < (1u << 22); ++spin) {
      unsigned long long f = tag;
      if (lane < 8) f = __hip_atomic_load(ctl + 16 * (lane + 1), __ATOMIC_RELAXED, __HIP_MEMORY_SCOPE_AGENT);
      if (__all(f == tag)) break;
      __builtin_amdgcn_s_sleep(2);
    }
    __builtin_amdgcn_fence(__ATOMIC_ACQUIRE, "wavefront");
    STAMP(6);
    const unsigned off = (unsigned)WS_PART + ((unsigned)(grp * 56 + kvh * 7 + w) << 13) + lane * 16;
    u32x4 v[8];
#pragma unroll
    for (int s = 0; s < 8; ++s) v[s] = __builtin_amdgcn_raw_buffer_load_b128(wsr, off + s * 1024, 0, 16);
    float acc[8];
#pragma unroll
    for (int e = 0; e < 8; ++e) acc[e] = 0.f;
#pragma unroll
    for (int s = 0; s < 8; ++s)
#pragma unroll
      for (int e = 0; e < 4; ++e) { acc[2 * e] += __uint_as_float(v[s][e] << 16); acc[2 * e + 1] += __uint_as_float(v[s][e] & 0xffff0000u); }
    const int q = 32 * (kvh >> 2) + li, n = 64 * w + 16 * (kvh & 3) + 4 * g;
    char* o = out + ((size_t)(b * SQ + q) * (2 * DM) + br * DM + n) * 2;
    *(u32x2*)o = u32x2{cvt2(acc[0], acc[1]), cvt2(acc[2], acc[3])};
    *(u32x2*)(o + 16 * 2 * DM * 2) = u32x2{cvt2(acc[4], acc[5]), cvt2(acc[6], acc[7])};
    STAMP(7);
    if (kvh == 0 && w == 0 && lane == 0) __hip_atomic_store(ctl, tag, __ATOMIC_RELAXED, __HIP_MEMORY_SCOPE_AGENT);
  }
}

extern "C" void kernel_launch(void* const* d_in, const int* in_sizes, int n_in, void* d_out, int out_size, void* d_ws, size_t ws_size,
                              hipStream_t stream) {
  (void)in_sizes; (void)n_in; (void)out_size; (void)ws_size;
  for (int i = 0; i < n_in; ++i) fprintf(stderr, "PTR in[%d]=%p\n", i, d_in[i]);
  fprintf(stderr, "PTR out=%p ws=%p\n", d_out, d_ws);
  (void)hipFuncSetAttribute((const void*)gqa_fused_kernel, hipFuncAttributeMaxDynamicSharedMemorySize, LDS_BYTES);
  gqa_fused_kernel<<<256, 512, LDS_BYTES, stream>>>(
      (const char*)d_in[0], (const char*)d_in[1], (const char*)d_in[2], (const char*)d_in[3], (const char*)d_in[4],
      (const char*)d_in[5], (const char*)d_in[6], (const char*)d_in[7], (const char*)d_in[8], (const char*)d_in[9],
      (char*)d_out, (char*)d_ws);
}
```

```cpp
#include <hip/hip_runtime.h>
#include <hip/hip_bf16.h>
#include <stdint.h>
#include <type_traits>
#define CODE_BYTES 44000u

typedef __attribute__((ext_vector_type(8))) short bf16x8;
typedef __attribute__((ext_vector_type(4))) short s16x4;
typedef __attribute__((ext_vector_type(4))) float f32x4;
typedef __attribute__((ext_vector_type(16))) float f32x16;
typedef __attribute__((ext_vector_type(2))) float f32x2;
typedef __attribute__((ext_vector_type(2))) __bf16 bf16x2_t;
typedef __attribute__((ext_vector_type(4))) unsigned u32x4;
typedef __attribute__((ext_vector_type(2))) unsigned u32x2;
#define LAS __attribute__((address_space(3)))
typedef LAS char* lds_ptr;

constexpr int SQ = 64, DM = 448, HKV = 8, SK = 1024, DH = 256;
constexpr int SS = 32768;
constexpr int MISC = 4 * SS;
constexpr int LDS_BYTES = 5 * SS;
constexpr int XIMG = 3 * SS;
constexpr int QST = 3 * SS;
constexpr float CEXP = 0.0625f * 1.4426950408889634f;
#define LMAX 1.0e18f
#define STAMP_ATT 0
#define NBK 5
#define NBV 6
#define NBW 4
constexpr int RDEF = 2;

constexpr size_t WS_PART = 65536;

#define MFMA16(a, b, c) __builtin_amdgcn_mfma_f32_16x16x32_bf16((a), (b), (c), 0, 0, 0)
#define MFMA32(a, b, c) __builtin_amdgcn_mfma_f32_32x32x16_bf16((a), (b), (c), 0, 0, 0)
#define CBAR() asm volatile("s_waitcnt lgkmcnt(0)\n\ts_barrier" ::: "memory")
#define SBAR() __builtin_amdgcn_sched_barrier(0)
#define OPAQUE(x) asm volatile("" : "+v"(x))
#define PIN2(x, y) asm volatile("" : "+v"(x), "+v"(y))
#define SOPAQUE(x) asm volatile("" : "+s"(x))
#define DI __device__ __forceinline__
template <int N> DI void wbar() { asm volatile("s_waitcnt vmcnt(%0) lgkmcnt(0)\n\ts_barrier" ::"i"(N) : "memory"); }
DI void bar() { asm volatile("s_barrier" ::: "memory"); }
template <int N> using IC = std::integral_constant<int, N>;

DI unsigned cvt2(float a, float b) { f32x2 v = {a, b}; return __builtin_bit_cast(unsigned, __builtin_convertvector(v, bf16x2_t)); }
DI bf16x8 ldsb128(unsigned a) { return *(const LAS bf16x8*)(uintptr_t)a; }
DI s16x4 ldstr(unsigned a) { return __builtin_amdgcn_ds_read_tr16_b64_v4i16((LAS s16x4*)(uintptr_t)a); }
DI bf16x8 cat8(s16x4 a, s16x4 b) { return __builtin_shufflevector(a, b, 0, 1, 2, 3, 4, 5, 6, 7); }
DI int lane_id() { int l; asm volatile("v_mbcnt_lo_u32_b32 %0, -1, 0\n\tv_mbcnt_hi_u32_b32 %0, -1, %0" : "=v"(l)); return l; }
DI float xsum(float v) {
  { auto r = __builtin_amdgcn_permlane16_swap(__float_as_uint(v), __float_as_uint(v), false, false); v = __uint_as_float(r[0]) + __uint_as_float(r[1]); }
  { auto r = __builtin_amdgcn_permlane32_swap(__float_as_uint(v), __float_as_uint(v), false, false); v = __uint_as_float(r[0]) + __uint_as_float(r[1]); }
  return v;
}
DI float xmax(float v) {
  { auto r = __builtin_amdgcn_permlane16_swap(__float_as_uint(v), __float_as_uint(v), false, false); v = fmaxf(__uint_as_float(r[0]), __uint_as_float(r[1])); }
  { auto r = __builtin_amdgcn_permlane32_swap(__float_as_uint(v), __float_as_uint(v), false, false); v = fmaxf(__uint_as_float(r[0]), __uint_as_float(r[1])); }
  return v;
}

DI void mm2(f32x4& c0, f32x4& c1, bf16x8 a, bf16x8 b0, bf16x8 b1) {
  asm volatile("v_mfma_f32_16x16x32_bf16 %0, %2, %3, %0\n\tv_mfma_f32_16x16x32_bf16 %1, %2, %4, %1" : "+v"(c0), "+v"(c1) : "v"(a), "v"(b0), "v"(b1));
}
DI void mm2z(f32x4& c0, f32x4& c1, bf16x8 a, bf16x8 b0, bf16x8 b1) {
  asm volatile("v_mfma_f32_16x16x32_bf16 %0, %2, %3, 0\n\tv_mfma_f32_16x16x32_bf16 %1, %2, %4, 0" : "=&v"(c0), "=&v"(c1) : "v"(a), "v"(b0), "v"(b1));
}
DI void mm2c(f32x4& c0, f32x4& c1, bf16x8 a, bf16x8 b0, bf16x8 b1, f32x4 i0, f32x4 i1) {
  asm volatile("v_mfma_f32_16x16x32_bf16 %0, %2, %3, %5\n\tv_mfma_f32_16x16x32_bf16 %1, %2, %4, %6" : "=&v"(c0), "=&v"(c1) : "v"(a), "v"(b0), "v"(b1), "v"(i0), "v"(i1));
}
#define MFMA_DRAIN() do { asm volatile("s_nop 7\n\ts_nop 1" ::: "memory"); SBAR(); } while (0)

DI void dma4(unsigned lds, const void* sbase, unsigned v0, unsigned v1, unsigned v2, unsigned v3) {
  unsigned keep;
  asm volatile("s_mov_b32 %0, m0\n\ts_mov_b32 m0, %1\n\ts_nop 3\n\t"
               "global_load_lds_dwordx4 %2, %6\n\tglobal_load_lds_dwordx4 %3, %6 offset:1024\n\t"
               "global_load_lds_dwordx4 %4, %6 offset:2048\n\tglobal_load_lds_dwordx4 %5, %6 offset:3072\n\ts_mov_b32 m0, %0"
               : "=&s"(keep) : "s"(lds), "v"(v0), "v"(v1), "v"(v2), "v"(v3), "s"(sbase) : "memory");
}
DI void dma2(unsigned lds, const void* sbase, unsigned v0, unsigned v1) {
  unsigned keep;
  asm volatile("s_mov_b32 %0, m0\n\ts_mov_b32 m0, %1\n\ts_nop 3\n\t"
               "global_load_lds_dwordx4 %2, %4\n\tglobal_load_lds_dwordx4 %3, %4 offset:1024\n\ts_mov_b32 m0, %0"
               : "=&s"(keep) : "s"(lds), "v"(v0), "v"(v1), "s"(sbase) : "memory");
}
DI void dma4s(unsigned lds, const char* s, size_t gstride, unsigned v) {
  unsigned keep;
  asm volatile("s_mov_b32 %0, m0\n\ts_mov_b32 m0, %1\n\ts_nop 3\n\tglobal_load_lds_dwordx4 %2, %3 nt\n\t"
               "s_add_u32 m0, m0, 0x2000\n\ts_nop 0\n\tglobal_load_lds_dwordx4 %2, %4 nt\n\t"
               "s_add_u32 m0, m0, 0x2000\n\ts_nop 0\n\tglobal_load_lds_dwordx4 %2, %5 nt\n\t"
               "s_add_u32 m0, m0, 0x2000\n\ts_nop 0\n\tglobal_load_lds_dwordx4 %2, %6 nt\n\ts_mov_b32 m0, %0"
               : "=&s"(keep) : "s"(lds), "v"(v), "s"(s), "s"(s + gstride), "s"(s + 2 * gstride), "s"(s + 3 * gstride) : "memory", "scc");
}

DI void dma4s_keep(unsigned lds, const char* s, size_t gstride, unsigned v) {
  unsigned keep;
  asm volatile("s_mov_b32 %0, m0\n\ts_mov_b32 m0, %1\n\ts_nop 3\n\tglobal_load_lds_dwordx4 %2, %3\n\t"
               "s_add_u32 m0, m0, 0x400\n\ts_nop 0\n\tglobal_load_lds_dwordx4 %2, %4\n\t"
               "s_add_u32 m0, m0, 0x400\n\ts_nop 0\n\tglobal_load_lds_dwordx4 %2, %5\n\t"
               "s_add_u32 m0, m0, 0x400\n\ts_nop 0\n\tglobal_load_lds_dwordx4 %2, %6\n\ts_mov_b32 m0, %0"
               : "=&s"(keep) : "s"(lds), "v"(v), "s"(s), "s"(s + gstride), "s"(s + 2 * gstride), "s"(s + 3 * gstride) : "memory", "scc");
}

template <int NF, int NB, int NI, class RD, class MM, class PREV, class MID, class HOOK>
DI void ring_step(bf16x8 (&carry)[RDEF], RD rd, MM mm, PREV prev, MID mid, HOOK hook) {
  static_assert(NI + RDEF <= NB, "");
  bf16x8 fb[NB];
#pragma unroll
  for (int f = 0; f < NI; ++f) fb[f] = rd(f);
  SBAR();
#pragma unroll
  for (int r = 0; r < RDEF; ++r) { prev(r, carry[r]); SBAR(); fb[NI + r] = rd(NI + r); SBAR(); }
  mid();
  SBAR();
#pragma unroll
  for (int f = NI + RDEF; f < NB; ++f) fb[f] = rd(f);
  SBAR();
#pragma unroll
  for (int f = 0; f < NF - RDEF; ++f) {
    mm(f, fb[f % NB]); SBAR();
    if (f + NB < NF) { fb[f % NB] = rd(f + NB); SBAR(); }
    hook(f); SBAR();
  }
#pragma unroll
  for (int r = 0; r < RDEF; ++r) carry[r] = fb[(NF - RDEF + r) % NB];
}

#define TAU 0
#define PACE(n) do { if (TAU && w == 7) { while ((unsigned)__builtin_amdgcn_s_memrealtime() - tk0 < (unsigned)((n) * TAU)) __builtin_amdgcn_s_sleep(1); } } while (0)
#define TS(k) do {} while (0)
#define STAMP(i) do {} while (0)

__global__ __launch_bounds__(512, 2) void gqa_fused_kernel(
    const char* __restrict__ xA, const char* __restrict__ kA, const char* __restrict__ vA, const char* __restrict__ wqA, const char* __restrict__ woA,
    const char* __restrict__ xB, const char* __restrict__ kB, const char* __restrict__ vB, const char* __restrict__ wqB, const char* __restrict__ woB,
    char* __restrict__ out, char* __restrict__ ws) {
  extern __shared__ __attribute__((aligned(16))) char smem[];
  const unsigned lds0 = (unsigned)(uintptr_t)(lds_ptr)smem;
  int w = __builtin_amdgcn_readfirstlane(threadIdx.x >> 6);
  const int id = blockIdx.x, br = (id >> 2) & 1, kvh = 2 * (id & 3) + ((id >> 3) & 1), b = id >> 4, grp = br * 16 + b;
  int rot7 = b % 7;
  int rg = w & 3, hf = w >> 2;
  STAMP(0);
  unsigned long long pc0; asm volatile("s_getpc_b64 %0" : "=s"(pc0));
  const char* kg = (br ? kB : kA) + (size_t)(b * HKV + kvh) * (SK * DH * 2);
  const char* vg = (br ? vB : vA) + (size_t)(b * HKV + kvh) * (SK * DH * 2);
  const char* wog = (br ? woB : woA) + (size_t)kvh * 512 * (DM * 2);

  auto k_eighth = [&](int t, int e) {
    const int ln = lane_id(), hi = ln >> 5;
    const char* kb = kg; SOPAQUE(kb);
    dma4s(lds0 + (t & 3) * SS + e * 1024, kb + (size_t)t * SS + e * 1024, 8192, hi * 512 + (((ln & 31) ^ (2 * e + hi)) << 4));
  };

  int vrot = (id >> 3) & 3;
  auto issue_vw = [&](int p) {
    const unsigned dst = lds0 + (p & 3) * SS + w * 1024;
    const int ln = lane_id();
    const bool isv = p < 20 || (p < 44 && !(p & 1));
    if (isv) {
      const int n = p < 20 ? p - 16 : 4 + ((p - 20) >> 1);
      const int r8 = ln >> 3;
      const char* vb = vg; SOPAQUE(vb);
      dma4s(dst, vb + (size_t)((n & 3) * 256 + 8 * w) * 512 + (((n >> 2) + vrot) & 3) * 128, 64 * 512, r8 * 512 + (((ln & 7) ^ (r8 & 6)) << 4));
    } else {
      const int k = p < 44 ? (p - 20) >> 1 : p - 32, r8 = ln >> 3;
      const char* wb = wog; SOPAQUE(wb);
      dma4s_keep(lds0 + (p & 3) * SS + w * 4096, wb + (size_t)(((k >> 1) & 1) * 256 + (((k >> 2) + vrot) & 3) * 64 + (k & 1) * 32) * 896 + (w < 7 ? w : 6) * 128,
                 8 * 896, r8 * 896 + (((ln & 7) ^ (r8 & 6)) << 4));
    }
  };
  auto issue = [&](int p) { if (p < 16) k_eighth(p, w); else issue_vw(p); };

  u32x4 pst[16][2];
  f32x4 oa[4][2];
  bf16x8 carry[RDEF];
  float mref[2] = {-INFINITY, -INFINITY};
  unsigned tk0;
  float cpk;

  auto vaddr = [&](int slot) {
    const int ln = lane_id(), rl = ((ln >> 4) << 2) | ((ln >> 2) & 3);
    unsigned a = lds0 + slot * SS + hf * 4096 + rl * 128 + ((((ln >> 1) & 1) ^ (rl & 6) ^ (hf << 2)) << 4) + (ln & 1) * 8;
    OPAQUE(a);
    return a;
  };
  auto vu = [](int c, int f) { return c == 0 ? ((f >> 1) & 3) : (f >> 2); };
  auto vd = [](int c, int f) { return c == 0 ? ((f & 1) | ((f & 8) ? 0 : 2)) : (f & 3); };
  auto vrd = [&](auto cc, unsigned vb, int f) {
    constexpr int c = decltype(cc)::value;
    const unsigned a = (vb ^ (unsigned)(vd(c, f) << 5)) + vu(c, f) * 8192; return cat8(ldstr(a), ldstr(a + 2048));
  };
  auto vmm = [&](auto cc, int f, bf16x8 a) {
    constexpr int c = decltype(cc)::value;
    const int u = vu(c, f), db = vd(c, f);
    const bf16x8 b0 = __builtin_bit_cast(bf16x8, pst[4 * c + u][0]), b1 = __builtin_bit_cast(bf16x8, pst[4 * c + u][1]);
    if (c == 0 && u == 0) mm2z(oa[db][0], oa[db][1], a, b0, b1); else mm2(oa[db][0], oa[db][1], a, b0, b1);
  };
  auto nohook = [](int) {};
  auto nop = []() {};

  for (int attempt = 0;; ++attempt) {
  SOPAQUE(w); SOPAQUE(rot7); SOPAQUE(vrot); rg = w & 3; hf = w >> 2;
  if (w >= 4) {
    const int lw = w - 4, lane = lane_id();
    const char* xg = (br ? xB : xA) + (size_t)b * (SQ * DM * 2); SOPAQUE(xg);
    const char* wqg = (br ? wqB : wqA) + (size_t)kvh * 1024; SOPAQUE(wqg);
    unsigned wofs[4];
#pragma unroll
    for (int j = 0; j < 4; ++j) wofs[j] = j * (8192 - 1024) + ((lane ^ (j << 2)) << 4);
    int wsl = 1;
    auto wtile = [&](int i) {
      int G = (i >> 1) + rot7; G = G >= 7 ? G - 7 : G;
      const char* src = wqg + (size_t)(G * 64 + (i & 1) * 32 + 8 * lw) * 8192;
      const unsigned dst = lds0 + wsl * SS + lw * 8192;
      dma4(dst, src, wofs[0], wofs[1], wofs[2], wofs[3]);
      dma4(dst + 4096, src + 4 * 8192, wofs[0], wofs[1], wofs[2], wofs[3]);
      wsl = wsl == 2 ? 0 : wsl + 1;
    };
    const int xr = 16 * lw + (lane >> 3), xc = lane & 7;
    const unsigned x0 = xr * 896 + ((xc ^ ((xr >> 1) & 7)) << 4), x1 = (xr + 8) * 896 + ((xc ^ (((xr + 8) >> 1) & 7)) << 4) - 1024;
    auto xtile = [&](int Gi) { int G = Gi + rot7; G = G >= 7 ? G - 7 : G; dma2(lds0 + XIMG + Gi * 8192 + lw * 2048, xg + G * 128, x0, x1); };
    xtile(0); wtile(0); wtile(1);
    for (int i = 0; i < 14; ++i) {
      if (i == 1) wbar<20>(); else wbar<8>();
      if (i == 0) for (int Gi = 1; Gi < 7; ++Gi) xtile(Gi);
      if (i + 2 < 14) wtile(i + 2); else { k_eighth(i - 12, 2 * lw); k_eighth(i - 12, 2 * lw + 1); }
    }
  } else {
    const int lane = lane_id(), g = lane >> 4, qq = (lane >> 2) & 3, pp = lane & 3;
    const int mh = w & 1, hh = w >> 1, h = lane >> 5, m = lane & 31;
    f32x16 qa[8];
#pragma unroll
    for (int a = 0; a < 8; ++a) qa[a] = (f32x16)(0.f);
    const unsigned wbase = lds0 + (8 * h + qq) * 1024 + hh * 512 + (((2 * (g & 1) + (pp >> 1)) ^ (qq << 2)) << 4) + (pp & 1) * 8;
    const unsigned xbase = lds0 + XIMG + (32 * mh + m) * 128 + ((h ^ ((m >> 1) & 7)) << 4);
    bf16x8 wfA[8], wfB[8], xa, xb, xo, xc = (bf16x8)(0), xd = (bf16x8)(0);
#pragma unroll
    for (int a = 0; a < 8; ++a) wfB[a] = (bf16x8)(0);
    xo = (bf16x8)(0);
    int sl = SS;
    unsigned tch = 0, tch2 = 0;
    asm volatile("global_load_dword %0, %1, %2" : "=v"(tch) : "v"((w * 64 + lane) * 128), "s"(pc0) : "memory");
    { unsigned o = 32768 + (w * 64 + lane) * 128; o = o < CODE_BYTES ? o : 0; asm volatile("global_load_dword %0, %1, %2" : "=v"(tch2) : "v"(o), "s"(pc0) : "memory"); }
    for (int i = 0; i < 14; ++i) {
      CBAR();
      unsigned p = wbase + sl; OPAQUE(p);
      sl = sl == 2 * SS ? 0 : sl + SS;
      const unsigned xp = (xbase + (i >> 1) * 8192) ^ (unsigned)((i & 1) << 6);
      if (i < 13) { xa = ldsb128(xp); xb = ldsb128(xp ^ 32); } else { xa = xc; xb = xd; }
      if (i == 12) { xc = ldsb128(xp ^ 64); xd = ldsb128(xp ^ 96); }
      SBAR();
#pragma unroll
      for (int a = 0; a < 8; ++a) {
        const unsigned q = (p ^ (unsigned)((a & 3) << 6)) + (a >> 2) * 256; wfA[a] = cat8(ldstr(q), ldstr(q + 4096));
        qa[a] = MFMA32(wfB[a], xo, qa[a]);
        SBAR();
      }
#pragma unroll
      for (int a = 0; a < 8; ++a) {
        if (a < 4) {
#pragma unroll
          for (int u = 0; u < 2; ++u) { const int a2 = 2 * a + u; const unsigned q = (p ^ (unsigned)((a2 & 3) << 6)) + (a2 >> 2) * 256; wfB[a2] = cat8(ldstr(q + 16384), ldstr(q + 16384 + 4096)); }
        }
        qa[a] = MFMA32(wfA[a], xa, qa[a]);
        SBAR();
      }
      xo = xb;
    }
    asm volatile("s_waitcnt vmcnt(0)" : "+v"(tch), "+v"(tch2) :: "memory");
    const unsigned qw = lds0 + QST + (hh * 2 + mh) * 16384 + (m >> 4) * 8192 + (m & 15) * 16 + h * 8;
#pragma unroll
    for (int nb = 0; nb < 8; ++nb) {
      qa[nb] = MFMA32(wfB[nb], xo, qa[nb]);
#pragma unroll
      for (int a = 0; a < 4; ++a)
        *(LAS u32x2*)(uintptr_t)(qw + nb * 1024 + a * 256) =
            u32x2{cvt2(qa[nb][4 * a] * CEXP, qa[nb][4 * a + 1] * CEXP), cvt2(qa[nb][4 * a + 2] * CEXP, qa[nb][4 * a + 3] * CEXP)};
    }
  }
  CBAR();
  bf16x8 qf[2][8];
  {
    const unsigned qr = lds0 + QST + rg * 16384 + lane_id() * 16;
#pragma unroll
    for (int qb = 0; qb < 2; ++qb)
#pragma unroll
      for (int s = 0; s < 8; ++s) qf[qb][s] = ldsb128(qr + (qb * 8 + s) * 1024);
  }
  if (attempt == STAMP_ATT) STAMP(1);

    tk0 = (unsigned)__builtin_amdgcn_s_memrealtime();
    f32x4 sa[2][2];
    f32x4 nm[2];
    float tmax[2] = {0.f, 0.f}, lsum[2] = {0.f, 0.f};
    auto kmm = [&](auto tt, int f, bf16x8 a) {
      constexpr int t = decltype(tt)::value;
      if (f >= 2) mm2(sa[f & 1][0], sa[f & 1][1], a, qf[0][f >> 1], qf[1][f >> 1]);
      else if (t == 0) mm2z(sa[f][0], sa[f][1], a, qf[0][0], qf[1][0]);
      else mm2c(sa[f][0], sa[f][1], a, qf[0][0], qf[1][0], nm[0], nm[1]);
    };
    auto smax = [&](int qb) {
      return fmaxf(fmaxf(fmaxf(sa[0][qb][0], sa[0][qb][1]), fmaxf(sa[0][qb][2], sa[0][qb][3])), fmaxf(fmaxf(sa[1][qb][0], sa[1][qb][1]), fmaxf(sa[1][qb][2], sa[1][qb][3])));
    };
    auto soft = [&](auto tt) {
      constexpr int t = decltype(tt)::value;
      MFMA_DRAIN();
#pragma unroll
      for (int qb = 0; qb < 2; ++qb) {
        if (t == 0) {
          mref[qb] = fmaxf(mref[qb], xmax(smax(qb)));
          nm[qb] = (f32x4)(-mref[qb]); OPAQUE(nm[qb]);
          sa[0][qb] += nm[qb]; sa[1][qb] += nm[qb];
        } else if (attempt == 1) tmax[qb] = fmaxf(tmax[qb], smax(qb));
        float e[8];
#pragma unroll
        for (int r = 0; r < 8; ++r) e[r] = __builtin_amdgcn_exp2f(sa[r >> 2][qb][r & 3]);
        lsum[qb] += ((e[0] + e[1]) + (e[2] + e[3])) + ((e[4] + e[5]) + (e[6] + e[7])); OPAQUE(lsum[qb]);
        pst[t][qb] = u32x4{cvt2(e[0], e[1]), cvt2(e[2], e[3]), cvt2(e[4], e[5]), cvt2(e[6], e[7])};
        OPAQUE(pst[t][qb]);
      }
    };
    auto kstep = [&](auto tt) {
      constexpr int t = decltype(tt)::value;
      const int ln = lane_id();
      unsigned kb0 = lds0 + (t & 3) * SS + hf * 16384 + (ln & 15) * 512 + (((ln >> 4) ^ (ln & 15)) << 4); OPAQUE(kb0);
      if (t > 1) PACE(t - 1);
      if (t == 0) wbar<8>(); else wbar<4>();
      if (hf == 1) issue(t + 2);
      ring_step<16, NBK, 2>(carry,
          [&](int f) { return ldsb128((kb0 ^ (unsigned)(((f >> 1) & 3) << 6)) + (f >> 3) * 256 + (f & 1) * 8192); },
          [&](int f, bf16x8 a) { kmm(IC<t>{}, f, a); },
          [&](int r, bf16x8 a) { if constexpr (t > 0) kmm(IC<t - 1>{}, 16 - RDEF + r, a); },
          [&]() { if constexpr (t > 0) soft(IC<t - 1>{}); }, nohook);
      if (hf == 0) issue(t + 2);
    };
    kstep(IC<0>{}); kstep(IC<1>{}); kstep(IC<2>{}); kstep(IC<3>{}); kstep(IC<4>{}); kstep(IC<5>{}); kstep(IC<6>{}); kstep(IC<7>{});
    kstep(IC<8>{}); kstep(IC<9>{}); kstep(IC<10>{}); kstep(IC<11>{}); kstep(IC<12>{}); kstep(IC<13>{}); kstep(IC<14>{}); kstep(IC<15>{});
    if (attempt == STAMP_ATT) STAMP(2);
    {
      const unsigned vb = vaddr(0); PACE(15); wbar<4>(); if (hf == 1) { issue_vw(18); issue_vw(19); }
      ring_step<16, NBV, 2>(carry, [&](int f) { return vrd(IC<0>{}, vb, f); }, [&](int f, bf16x8 a) { vmm(IC<0>{}, f, a); },
                            [&](int r, bf16x8 a) { kmm(IC<15>{}, 16 - RDEF + r, a); },
                            [&]() {
                              soft(IC<15>{});
                              const int ln = lane_id();
                              lsum[0] = xsum(lsum[0]); lsum[1] = xsum(lsum[1]);
                              const bool bad = !(lsum[0] <= LMAX) || !(lsum[1] <= LMAX);
                              const float anybad = __any(bad) ? 1.f : 0.f;
                              if (ln < 16) *(LAS f32x4*)(uintptr_t)(lds0 + MISC + w * 256 + ln * 16) = f32x4{mref[0], lsum[0], mref[1], lsum[1]};
                              if (ln == 0) *(LAS float*)(uintptr_t)(lds0 + MISC + 2048 + w * 4) = anybad;
                            }, nohook);
      if (hf == 0) { issue_vw(18); issue_vw(19); }
    }
    float fl;
    {
      const unsigned vb = vaddr(1); PACE(16); wbar<8>(); if (hf == 1) issue_vw(20);
      ring_step<16, NBV, 2>(carry, [&](int f) { return vrd(IC<1>{}, vb, f); }, [&](int f, bf16x8 a) { vmm(IC<1>{}, f, a); },
                         [&](int r, bf16x8 a) { vmm(IC<0>{}, 16 - RDEF + r, a); },
                         [&]() {
                           const int ln = lane_id();
                           const f32x4 o = *(const LAS f32x4*)(uintptr_t)(lds0 + MISC + (w ^ 4) * 256 + (ln & 15) * 16);
                           const f32x4 f0 = *(const LAS f32x4*)(uintptr_t)(lds0 + MISC + 2048), f1 = *(const LAS f32x4*)(uintptr_t)(lds0 + MISC + 2048 + 16);
                           fl = (f0[0] + f0[1]) + (f0[2] + f0[3]) + (f1[0] + f1[1]) + (f1[2] + f1[3]); OPAQUE(fl);
                           const bool odd = (ln >> 4) & 1;
                           float l0 = lsum[0], l1 = lsum[1], m0 = mref[0], m1 = mref[1], o0_ = o[0], o1_ = o[1], o2_ = o[2], o3_ = o[3];
                           OPAQUE(l0); OPAQUE(l1); OPAQUE(m0); OPAQUE(m1); OPAQUE(o0_); OPAQUE(o1_); OPAQUE(o2_); OPAQUE(o3_);
                           const float ms = odd ? m1 : m0, ls = odd ? l1 : l0, mo = odd ? o2_ : o0_, lo = odd ? o3_ : o1_;
                           const float m = fmaxf(ms, mo), ea = __builtin_amdgcn_exp2f(ms - m), eb = __builtin_amdgcn_exp2f(mo - m);
                           cpk = ea / (ls * ea + lo * eb); OPAQUE(cpk);
                         }, nohook);
      if (hf == 0) issue_vw(20);
    }
    if (attempt == 2 || (attempt == 0 && __builtin_amdgcn_readfirstlane(__float_as_uint(fl)) == 0u)) break;
    mref[0] += xmax(tmax[0]); mref[1] += xmax(tmax[1]);
    wbar<0>();
  }
  {
    const unsigned vb = vaddr(2); PACE(17); wbar<8>(); if (hf == 1) issue_vw(21);
    ring_step<16, NBV, 2>(carry, [&](int f) { return vrd(IC<2>{}, vb, f); }, [&](int f, bf16x8 a) { vmm(IC<2>{}, f, a); },
                          [&](int r, bf16x8 a) { vmm(IC<1>{}, 16 - RDEF + r, a); }, nop, nohook);
    if (hf == 0) issue_vw(21);
  }
  {
    const unsigned vb = vaddr(3); PACE(18); wbar<8>(); if (hf == 1) issue_vw(22);
    ring_step<16, NBV, 2>(carry, [&](int f) { return vrd(IC<3>{}, vb, f); }, [&](int f, bf16x8 a) { vmm(IC<3>{}, f, a); },
                          [&](int r, bf16x8 a) { vmm(IC<2>{}, 16 - RDEF + r, a); }, nop, nohook);
  }

  const int qh = w >> 2, nq = w & 3;
  f32x4 pa[7][2];
#pragma unroll
  for (int i = 0; i < 7; ++i) { pa[i][0] = (f32x4)(0.f); pa[i][1] = (f32x4)(0.f); }
  bf16x8 o0 = (bf16x8)(0), o1 = (bf16x8)(0);
  auto oload = [&](int idx) {
    const unsigned oad = lds0 + MISC + ((((idx >> 1) * 2 + qh) + 4 * ((idx & 1) ^ 1)) * 4096) + lane_id() * 16;
    o0 = ldsb128(oad); o1 = ldsb128(oad + 1024);
  };
  auto wmm = [&](int i, bf16x8 a) { mm2(pa[i][0], pa[i][1], a, o0, o1); };
  float c0, c1;
  auto comb_write = [&](int k) {
    if (k == 0) {
      float cc = cpk; OPAQUE(cc);
      const auto cr = __builtin_amdgcn_permlane16_swap(__float_as_uint(cc), __float_as_uint(cc), false, false);
      c0 = __uint_as_float(cr[0]); c1 = __uint_as_float(cr[1]);
    }
    if (k < 2) {
      const unsigned ad = lds0 + MISC + w * 4096 + k * 2048 + lane_id() * 16;
      *(LAS f32x4*)(uintptr_t)(ad) = oa[2 + k][0] * c0; *(LAS f32x4*)(uintptr_t)(ad + 1024) = oa[2 + k][1] * c1;
    } else if (k < 4) { oa[k - 2][0] *= c0; oa[k - 2][1] *= c1; PIN2(oa[k - 2][0], oa[k - 2][1]); }
  };
  f32x4 ct0, ct1; u32x4 cf0;
  auto comb_rd = [&](int qb) {
    const unsigned pad = lds0 + MISC + (w ^ 4) * 4096 + qb * 1024 + lane_id() * 16;
    ct0 = *(const LAS f32x4*)(uintptr_t)(pad); ct1 = *(const LAS f32x4*)(uintptr_t)(pad + 2048);
  };
  auto comb_fin = [&](int qb) {
    const f32x4 a = ct0 + oa[0][qb], c = ct1 + oa[1][qb];
    *(LAS u32x4*)(uintptr_t)(lds0 + MISC + (w ^ 4) * 4096 + qb * 1024 + lane_id() * 16) = u32x4{cvt2(a[0], a[1]), cvt2(a[2], a[3]), cvt2(c[0], c[1]), cvt2(c[2], c[3])};
  };
  auto vstep = [&](int slot, int pn, auto cc) {
    constexpr int c = decltype(cc)::value;
    const unsigned vb = vaddr(slot);
    wbar<8>(); TS(c == 0 ? 2 : 1);
    if (hf == 1) issue_vw(pn);
    ring_step<16, (c == 0 ? NBV - 1 : NBV), 2>(carry, [&](int f) { return vrd(cc, vb, f); }, [&](int f, bf16x8 a) { vmm(cc, f, a); },
                          [&](int r, bf16x8 a) { wmm(7 - RDEF + r, a); }, [&]() { if (c == 0) comb_rd(0); },
                          [&](int f) { if (c == 0 && f == 2) { comb_fin(0); comb_rd(1); } if (c == 0 && f == 6) comb_fin(1); });
  };
  auto wstep = [&](int slot, auto pre, auto prev, auto mid, auto hook) {
    const int ln = lane_id(), rl = ((ln >> 4) << 2) | ((ln >> 2) & 3);
    unsigned wb = lds0 + slot * SS + rl * 128 + (((2 * nq + ((ln >> 1) & 1)) ^ (rl & 6)) << 4) + (ln & 1) * 8;
    OPAQUE(wb);
    pre();
    ring_step<7, NBW, NBW - RDEF>(carry, [&](int i) { return cat8(ldstr(wb + i * 4096), ldstr(wb + i * 4096 + 2048)); }, wmm, prev, mid, hook);
  };
#pragma unroll
  for (int r = 0; r < RDEF; ++r) { vmm(IC<3>{}, 16 - RDEF + r, carry[r]); carry[r] = (bf16x8)(0); }
  if (hf == 0) issue_vw(22);
  MFMA_DRAIN();
  comb_write(0); comb_write(1); comb_write(2); comb_write(3);
#pragma unroll 1
  for (int p = 20; p < 44; p += 8) {
    vstep(0, p + 3, IC<0>{}); if (hf == 0) issue_vw(p + 3);
    wstep(1, [&]() { wbar<8>(); TS(3); if (hf == 1) issue_vw(p + 4); oload(0); }, [&](int r, bf16x8 a) { vmm(IC<0>{}, 16 - RDEF + r, a); }, nop, nohook); if (hf == 0) issue_vw(p + 4);
    vstep(2, p + 5, IC<1>{}); if (hf == 0) issue_vw(p + 5);
    wstep(3, [&]() { wbar<8>(); TS(0); if (hf == 1) issue_vw(p + 6); oload(1); }, [&](int r, bf16x8 a) { vmm(IC<1>{}, 16 - RDEF + r, a); }, nop, nohook); if (hf == 0) issue_vw(p + 6);
    vstep(0, p + 7, IC<2>{}); if (hf == 0) issue_vw(p + 7);
    wstep(1, [&]() { wbar<8>(); TS(0); if (hf == 1) issue_vw(p + 8); oload(2); }, [&](int r, bf16x8 a) { vmm(IC<2>{}, 16 - RDEF + r, a); }, nop, nohook); if (hf == 0) issue_vw(p + 8);
    vstep(2, p + 9, IC<3>{}); oload(3); if (hf == 0) issue_vw(p + 9);
    wstep(3, [&]() { wbar<8>(); TS(0); if (hf == 1) issue_vw(p + 10); }, [&](int r, bf16x8 a) { vmm(IC<3>{}, 16 - RDEF + r, a); }, nop,
          [&](int f) { if (f >= 1) comb_write(f - 1); }); if (hf == 0) issue_vw(p + 10);
  }
  STAMP(3);
  CBAR();
  comb_rd(0);
#pragma unroll
  for (int r = 0; r < RDEF; ++r) wmm(7 - RDEF + r, carry[r]);
  comb_fin(0); comb_rd(1); issue_vw(47); comb_fin(1);
  auto wprev = [&](int r, bf16x8 a) { wmm(7 - RDEF + r, a); };
  unsigned long long* ctl = (unsigned long long*)(ws + (size_t)grp * 2048);
  unsigned long long ep;
  wstep(0, [&]() { wbar<8>(); oload(0); }, [&](int, bf16x8) {}, nop, nohook);
  wstep(1, nop, wprev, [&]() { oload(1); }, nohook);
  wstep(2, [&]() { wbar<0>(); ep = __hip_atomic_load(ctl, __ATOMIC_RELAXED, __HIP_MEMORY_SCOPE_AGENT); }, wprev, [&]() { oload(2); }, nohook);
  wstep(3, nop, wprev, [&]() { oload(3); }, nohook);
#pragma unroll
  for (int r = 0; r < RDEF; ++r) wmm(7 - RDEF + r, carry[r]);
  MFMA_DRAIN();
  const int lane = lane_id(), g = lane >> 4, li = lane & 15;
  const __amdgpu_buffer_rsrc_t wsr = __builtin_amdgcn_make_buffer_rsrc(ws, 0, 0x7fffffff, 0x00020000);
#pragma unroll
  for (int i = 0; i < 7; ++i) {
    u32x4 f = {cvt2(pa[i][0][0], pa[i][0][1]), cvt2(pa[i][0][2], pa[i][0][3]), cvt2(pa[i][1][0], pa[i][1][1]), cvt2(pa[i][1][2], pa[i][1][3])};
    const unsigned off = (unsigned)WS_PART + (((unsigned)(grp * 56 + w * 7 + i) * 8 + kvh) << 10) + lane * 16;
    __builtin_amdgcn_raw_buffer_store_b128(f, wsr, off, 0, 16);
  }
  const unsigned long long tag = ep * 0x9E3779B97F4A7C15ull + 0x632BE59BD9B4E019ull;
  STAMP(4);
  wbar<0>();
  STAMP(5);
  if (w == 0 && lane == 0) __hip_atomic_store(ctl + 16 * (kvh + 1), tag, __ATOMIC_RELAXED, __HIP_MEMORY_SCOPE_AGENT);
  if (w < 7) {
    for (unsigned spin = 0; spin < (1u << 22); ++spin) {
      unsigned long long f = tag;
      if (lane < 8) f = __hip_atomic_load(ctl + 16 * (lane + 1), __ATOMIC_RELAXED, __HIP_MEMORY_SCOPE_AGENT);
      if (__all(f == tag)) break;
      __builtin_amdgcn_s_sleep(2);
    }
    __builtin_amdgcn_fence(__ATOMIC_ACQUIRE, "wavefront");
    STAMP(6);
    const unsigned off = (unsigned)WS_PART + ((unsigned)(grp * 56 + kvh * 7 + w) << 13) + lane * 16;
    u32x4 v[8];
#pragma unroll
    for (int s = 0; s < 8; ++s) v[s] = __builtin_amdgcn_raw_buffer_load_b128(wsr, off + s * 1024, 0, 16);
    float acc[8];
#pragma unroll
    for (int e = 0; e < 8; ++e) acc[e] = 0.f;
#pragma unroll
    for (int s = 0; s < 8; ++s)
#pragma unroll
      for (int e = 0; e < 4; ++e) { acc[2 * e] += __uint_as_float(v[s][e] << 16); acc[2 * e + 1] += __uint_as_float(v[s][e] & 0xffff0000u); }
    const int q = 32 * (kvh >> 2) + li, n = 64 * w + 16 * (kvh & 3) + 4 * g;
    char* o = out + ((size_t)(b * SQ + q) * (2 * DM) + br * DM + n) * 2;
    *(u32x2*)o = u32x2{cvt2(acc[0], acc[1]), cvt2(acc[2], acc[3])};
    *(u32x2*)(o + 16 * 2 * DM * 2) = u32x2{cvt2(acc[4], acc[5]), cvt2(acc[6], acc[7])};
    STAMP(7);
    if (kvh == 0 && w == 0 && lane == 0) __hip_atomic_store(ctl, tag, __ATOMIC_RELAXED, __HIP_MEMORY_SCOPE_AGENT);
  }
}

extern "C" void kernel_launch(void* const* d_in, const int* in_sizes, int n_in, void* d_out, int out_size, void* d_ws, size_t ws_size,
                              hipStream_t stream) {
  (void)in_sizes; (void)n_in; (void)out_size; (void)ws_size;
  (void)hipFuncSetAttribute((const void*)gqa_fused_kernel, hipFuncAttributeMaxDynamicSharedMemorySize, LDS_BYTES);
  gqa_fused_kernel<<<256, 512, LDS_BYTES, stream>>>(
      (const char*)d_in[0], (const char*)d_in[1], (const char*)d_in[2], (const char*)d_in[3], (const char*)d_in[4],
      (const char*)d_in[5], (const char*)d_in[6], (const char*)d_in[7], (const char*)d_in[8], (const char*)d_in[9],
      (char*)d_out, (char*)d_ws);
}
```

```cpp
#include <hip/hip_runtime.h>
#include <hip/hip_bf16.h>
#include <stdint.h>
#include <type_traits>

typedef __attribute__((ext_vector_type(8))) short bf16x8;
typedef __attribute__((ext_vector_type(4))) short s16x4;
typedef __attribute__((ext_vector_type(4))) float f32x4;
typedef __attribute__((ext_vector_type(16))) float f32x16;
typedef __attribute__((ext_vector_type(2))) float f32x2;
typedef __attribute__((ext_vector_type(2))) __bf16 bf16x2_t;
typedef __attribute__((ext_vector_type(4))) unsigned u32x4;
typedef __attribute__((ext_vector_type(2))) unsigned u32x2;
#define LAS __attribute__((address_space(3)))
typedef LAS char* lds_ptr;

constexpr int SQ = 64, DM = 448, HKV = 8, SK = 1024, DH = 256;
constexpr int SS = 32768;
constexpr int MISC = 4 * SS;
constexpr int LDS_BYTES = 5 * SS;
constexpr int XIMG = 3 * SS;
constexpr int QST = 3 * SS;
constexpr float CEXP = 0.0625f * 1.4426950408889634f;
#define LMAX 1.0e18f
#define STAMP_ATT 0
#define NBK 5
#define NBV 6
#define NBW 4
constexpr int RDEF = 2;

constexpr size_t WS_PART = 65536;

#define MFMA16(a, b, c) __builtin_amdgcn_mfma_f32_16x16x32_bf16((a), (b), (c), 0, 0, 0)
#define MFMA32(a, b, c) __builtin_amdgcn_mfma_f32_32x32x16_bf16((a), (b), (c), 0, 0, 0)
#define CBAR() asm volatile("s_waitcnt lgkmcnt(0)\n\ts_barrier" ::: "memory")
#define SBAR() __builtin_amdgcn_sched_barrier(0)
#define OPAQUE(x) asm volatile("" : "+v"(x))
#define PIN2(x, y) asm volatile("" : "+v"(x), "+v"(y))
#define SOPAQUE(x) asm volatile("" : "+s"(x))
#define DI __device__ __forceinline__
template <int N> DI void wbar() { asm volatile("s_waitcnt vmcnt(%0) lgkmcnt(0)\n\ts_barrier" ::"i"(N) : "memory"); }
DI void bar() { asm volatile("s_barrier" ::: "memory"); }
template <int N> using IC = std::integral_constant<int, N>;

DI unsigned cvt2(float a, float b) { f32x2 v = {a, b}; return __builtin_bit_cast(unsigned, __builtin_convertvector(v, bf16x2_t)); }
DI bf16x8 ldsb128(unsigned a) { return *(const LAS bf16x8*)(uintptr_t)a; }
DI s16x4 ldstr(unsigned a) { return __builtin_amdgcn_ds_read_tr16_b64_v4i16((LAS s16x4*)(uintptr_t)a); }
DI bf16x8 cat8(s16x4 a, s16x4 b) { return __builtin_shufflevector(a, b, 0, 1, 2, 3, 4, 5, 6, 7); }
DI int lane_id() { int l; asm volatile("v_mbcnt_lo_u32_b32 %0, -1, 0\n\tv_mbcnt_hi_u32_b32 %0, -1, %0" : "=v"(l)); return l; }
DI float xsum(float v) {
  { auto r = __builtin_amdgcn_permlane16_swap(__float_as_uint(v), __float_as_uint(v), false, false); v = __uint_as_float(r[0]) + __uint_as_float(r[1]); }
  { auto r = __builtin_amdgcn_permlane32_swap(__float_as_uint(v), __float_as_uint(v), false, false); v = __uint_as_float(r[0]) + __uint_as_float(r[1]); }
  return v;
}
DI float xmax(float v) {
  { auto r = __builtin_amdgcn_permlane16_swap(__float_as_uint(v), __float_as_uint(v), false, false); v = fmaxf(__uint_as_float(r[0]), __uint_as_float(r[1])); }
  { auto r = __builtin_amdgcn_permlane32_swap(__float_as_uint(v), __float_as_uint(v), false, false); v = fmaxf(__uint_as_float(r[0]), __uint_as_float(r[1])); }
  return v;
}

DI void mm2(f32x4& c0, f32x4& c1, bf16x8 a, bf16x8 b0, bf16x8 b1) {
  asm volatile("v_mfma_f32_16x16x32_bf16 %0, %2, %3, %0\n\tv_mfma_f32_16x16x32_bf16 %1, %2, %4, %1" : "+v"(c0), "+v"(c1) : "v"(a), "v"(b0), "v"(b1));
}
DI void mm2z(f32x4& c0, f32x4& c1, bf16x8 a, bf16x8 b0, bf16x8 b1) {
  asm volatile("v_mfma_f32_16x16x32_bf16 %0, %2, %3, 0\n\tv_mfma_f32_16x16x32_bf16 %1, %2, %4, 0" : "=&v"(c0), "=&v"(c1) : "v"(a), "v"(b0), "v"(b1));
}
DI void mm2c(f32x4& c0, f32x4& c1, bf16x8 a, bf16x8 b0, bf16x8 b1, f32x4 i0, f32x4 i1) {
  asm volatile("v_mfma_f32_16x16x32_bf16 %0, %2, %3, %5\n\tv_mfma_f32_16x16x32_bf16 %1, %2, %4, %6" : "=&v"(c0), "=&v"(c1) : "v"(a), "v"(b0), "v"(b1), "v"(i0), "v"(i1));
}
#define MFMA_DRAIN() do { asm volatile("s_nop 7\n\ts_nop 1" ::: "memory"); SBAR(); } while (0)

DI void dma4(unsigned lds, const void* sbase, unsigned v0, unsigned v1, unsigned v2, unsigned v3) {
  unsigned keep;
  asm volatile("s_mov_b32 %0, m0\n\ts_mov_b32 m0, %1\n\ts_nop 3\n\t"
               "global_load_lds_dwordx4 %2, %6\n\tglobal_load_lds_dwordx4 %3, %6 offset:1024\n\t"
               "global_load_lds_dwordx4 %4, %6 offset:2048\n\tglobal_load_lds_dwordx4 %5, %6 offset:3072\n\ts_mov_b32 m0, %0"
               : "=&s"(keep) : "s"(lds), "v"(v0), "v"(v1), "v"(v2), "v"(v3), "s"(sbase) : "memory");
}
DI void dma2(unsigned lds, const void* sbase, unsigned v0, unsigned v1) {
  unsigned keep;
  asm volatile("s_mov_b32 %0, m0\n\ts_mov_b32 m0, %1\n\ts_nop 3\n\t"
               "global_load_lds_dwordx4 %2, %4\n\tglobal_load_lds_dwordx4 %3, %4 offset:1024\n\ts_mov_b32 m0, %0"
               : "=&s"(keep) : "s"(lds), "v"(v0), "v"(v1), "s"(sbase) : "memory");
}
DI void dma4s(unsigned lds, const char* s, size_t gstride, unsigned v) {
  unsigned keep;
  asm volatile("s_mov_b32 %0, m0\n\ts_mov_b32 m0, %1\n\ts_nop 3\n\tglobal_load_lds_dwordx4 %2, %3 nt\n\t"
               "s_add_u32 m0, m0, 0x2000\n\ts_nop 0\n\tglobal_load_lds_dwordx4 %2, %4 nt\n\t"
               "s_add_u32 m0, m0, 0x2000\n\ts_nop 0\n\tglobal_load_lds_dwordx4 %2, %5 nt\n\t"
               "s_add_u32 m0, m0, 0x2000\n\ts_nop 0\n\tglobal_load_lds_dwordx4 %2, %6 nt\n\ts_mov_b32 m0, %0"
               : "=&s"(keep) : "s"(lds), "v"(v), "s"(s), "s"(s + gstride), "s"(s + 2 * gstride), "s"(s + 3 * gstride) : "memory", "scc");
}

DI void dma4s_keep(unsigned lds, const char* s, size_t gstride, unsigned v) {
  unsigned keep;
  asm volatile("s_mov_b32 %0, m0\n\ts_mov_b32 m0, %1\n\ts_nop 3\n\tglobal_load_lds_dwordx4 %2, %3\n\t"
               "s_add_u32 m0, m0, 0x400\n\ts_nop 0\n\tglobal_load_lds_dwordx4 %2, %4\n\t"
               "s_add_u32 m0, m0, 0x400\n\ts_nop 0\n\tglobal_load_lds_dwordx4 %2, %5\n\t"
               "s_add_u32 m0, m0, 0x400\n\ts_nop 0\n\tglobal_load_lds_dwordx4 %2, %6\n\ts_mov_b32 m0, %0"
               : "=&s"(keep) : "s"(lds), "v"(v), "s"(s), "s"(s + gstride), "s"(s + 2 * gstride), "s"(s + 3 * gstride) : "memory", "scc");
}

template <int NF, int NB, int NI, class RD, class MM, class PREV, class MID, class HOOK>
DI void ring_step(bf16x8 (&carry)[RDEF], RD rd, MM mm, PREV prev, MID mid, HOOK hook) {
  static_assert(NI + RDEF <= NB, "");
  bf16x8 fb[NB];
#pragma unroll
  for (int f = 0; f < NI; ++f) fb[f] = rd(f);
  SBAR();
#pragma unroll
  for (int r = 0; r < RDEF; ++r) { prev(r, carry[r]); SBAR(); fb[NI + r] = rd(NI + r); SBAR(); }
  mid();
  SBAR();
#pragma unroll
  for (int f = NI + RDEF; f < NB; ++f) fb[f] = rd(f);
  SBAR();
#pragma unroll
  for (int f = 0; f < NF - RDEF; ++f) {
    mm(f, fb[f % NB]); SBAR();
    if (f + NB < NF) { fb[f % NB] = rd(f + NB); SBAR(); }
    hook(f); SBAR();
  }
#pragma unroll
  for (int r = 0; r < RDEF; ++r) carry[r] = fb[(NF - RDEF + r) % NB];
}

#define TAU 0
#define PACE(n) do { if (TAU && w == 7) { while ((unsigned)__builtin_amdgcn_s_memrealtime() - tk0 < (unsigned)((n) * TAU)) __builtin_amdgcn_s_sleep(1); } } while (0)
#define TS(k) do {} while (0)
#define STAMP(i) do {} while (0)

__global__ __launch_bounds__(512, 2) void gqa_fused_kernel(
    const char* __restrict__ xA, const char* __restrict__ kA, const char* __restrict__ vA, const char* __restrict__ wqA, const char* __restrict__ woA,
    const char* __restrict__ xB, const char* __restrict__ kB, const char* __restrict__ vB, const char* __restrict__ wqB, const char* __restrict__ woB,
    char* __restrict__ out, char* __restrict__ ws) {
  extern __shared__ __attribute__((aligned(16))) char smem[];
  const unsigned lds0 = (unsigned)(uintptr_t)(lds_ptr)smem;
  int w = __builtin_amdgcn_readfirstlane(threadIdx.x >> 6);
  const int id = blockIdx.x, br = (id >> 2) & 1, kvh = 2 * (id & 3) + ((id >> 3) & 1), b = id >> 4, grp = br * 16 + b;
  int rot7 = b % 7;
  int rg = w & 3, hf = w >> 2;
  STAMP(0);
  unsigned long long pc0; unsigned code_len;
  asm volatile("s_getpc_b64 %0\ngqa_head_label:\n\ts_mov_b32 %1, gqa_tail_label-gqa_head_label" : "=s"(pc0), "=s"(code_len));
  const char* kg = (br ? kB : kA) + (size_t)(b * HKV + kvh) * (SK * DH * 2);
  const char* vg = (br ? vB : vA) + (size_t)(b * HKV + kvh) * (SK * DH * 2);
  const char* wog = (br ? woB : woA) + (size_t)kvh * 512 * (DM * 2);

  auto k_eighth = [&](int t, int e) {
    const int ln = lane_id(), hi = ln >> 5;
    const char* kb = kg; SOPAQUE(kb);
    dma4s(lds0 + (t & 3) * SS + e * 1024, kb + (size_t)t * SS + e * 1024, 8192, hi * 512 + (((ln & 31) ^ (2 * e + hi)) << 4));
  };

  int vrot = (id >> 3) & 3;
  auto issue_vw = [&](int p) {
    const unsigned dst = lds0 + (p & 3) * SS + w * 1024;
    const int ln = lane_id();
    const bool isv = p < 20 || (p < 44 && !(p & 1));
    if (isv) {
      const int n = p < 20 ? p - 16 : 4 + ((p - 20) >> 1);
      const int r8 = ln >> 3;
      const char* vb = vg; SOPAQUE(vb);
      dma4s(dst, vb + (size_t)((n & 3) * 256 + 8 * w) * 512 + (((n >> 2) + vrot) & 3) * 128, 64 * 512, r8 * 512 + (((ln & 7) ^ (r8 & 6)) << 4));
    } else {
      const int k = p < 44 ? (p - 20) >> 1 : p - 32, r8 = ln >> 3;
      const char* wb = wog; SOPAQUE(wb);
      dma4s_keep(lds0 + (p & 3) * SS + w * 4096, wb + (size_t)(((k >> 1) & 1) * 256 + (((k >> 2) + vrot) & 3) * 64 + (k & 1) * 32) * 896 + (w < 7 ? w : 6) * 128,
                 8 * 896, r8 * 896 + (((ln & 7) ^ (r8 & 6)) << 4));
    }
  };
  auto issue = [&](int p) { if (p < 16) k_eighth(p, w); else issue_vw(p); };

  u32x4 pst[16][2];
  f32x4 oa[4][2];
  bf16x8 carry[RDEF];
  float mref[2] = {-INFINITY, -INFINITY};
  unsigned tk0;
  float cpk;

  auto vaddr = [&](int slot) {
    const int ln = lane_id(), rl = ((ln >> 4) << 2) | ((ln >> 2) & 3);
    unsigned a = lds0 + slot * SS + hf * 4096 + rl * 128 + ((((ln >> 1) & 1) ^ (rl & 6) ^ (hf << 2)) << 4) + (ln & 1) * 8;
    OPAQUE(a);
    return a;
  };
  auto vu = [](int c, int f) { return c == 0 ? ((f >> 1) & 3) : (f >> 2); };
  auto vd = [](int c, int f) { return c == 0 ? ((f & 1) | ((f & 8) ? 0 : 2)) : (f & 3); };
  auto vrd = [&](auto cc, unsigned vb, int f) {
    constexpr int c = decltype(cc)::value;
    const unsigned a = (vb ^ (unsigned)(vd(c, f) << 5)) + vu(c, f) * 8192; return cat8(ldstr(a), ldstr(a + 2048));
  };
  auto vmm = [&](auto cc, int f, bf16x8 a) {
    constexpr int c = decltype(cc)::value;
    const int u = vu(c, f), db = vd(c, f);
    const bf16x8 b0 = __builtin_bit_cast(bf16x8, pst[4 * c + u][0]), b1 = __builtin_bit_cast(bf16x8, pst[4 * c + u][1]);
    if (c == 0 && u == 0) mm2z(oa[db][0], oa[db][1], a, b0, b1); else mm2(oa[db][0], oa[db][1], a, b0, b1);
  };
  auto nohook = [](int) {};
  auto nop = []() {};

  for (int attempt = 0;; ++attempt) {
  SOPAQUE(w); SOPAQUE(rot7); SOPAQUE(vrot); rg = w & 3; hf = w >> 2;
  if (w >= 4) {
    const int lw = w - 4, lane = lane_id();
    const char* xg = (br ? xB : xA) + (size_t)b * (SQ * DM * 2); SOPAQUE(xg);
    const char* wqg = (br ? wqB : wqA) + (size_t)kvh * 1024; SOPAQUE(wqg);
    unsigned wofs[4];
#pragma unroll
    for (int j = 0; j < 4; ++j) wofs[j] = j * (8192 - 1024) + ((lane ^ (j << 2)) << 4);
    int wsl = 1;
    auto wtile = [&](int i) {
      int G = (i >> 1) + rot7; G = G >= 7 ? G - 7 : G;
      const char* src = wqg + (size_t)(G * 64 + (i & 1) * 32 + 8 * lw) * 8192;
      const unsigned dst = lds0 + wsl * SS + lw * 8192;
      dma4(dst, src, wofs[0], wofs[1], wofs[2], wofs[3]);
      dma4(dst + 4096, src + 4 * 8192, wofs[0], wofs[1], wofs[2], wofs[3]);
      wsl = wsl == 2 ? 0 : wsl + 1;
    };
    const int xr = 16 * lw + (lane >> 3), xc = lane & 7;
    const unsigned x0 = xr * 896 + ((xc ^ ((xr >> 1) & 7)) << 4), x1 = (xr + 8) * 896 + ((xc ^ (((xr + 8) >> 1) & 7)) << 4) - 1024;
    auto xtile = [&](int Gi) { int G = Gi + rot7; G = G >= 7 ? G - 7 : G; dma2(lds0 + XIMG + Gi * 8192 + lw * 2048, xg + G * 128, x0, x1); };
    xtile(0); wtile(0); wtile(1);
    for (int i = 0; i < 14; ++i) {
      if (i == 1) wbar<20>(); else wbar<8>();
      if (i == 0) for (int Gi = 1; Gi < 7; ++Gi) xtile(Gi);
      if (i + 2 < 14) wtile(i + 2); else { k_eighth(i - 12, 2 * lw); k_eighth(i - 12, 2 * lw + 1); }
    }
  } else {
    const int lane = lane_id(), g = lane >> 4, qq = (lane >> 2) & 3, pp = lane & 3;
    const int mh = w & 1, hh = w >> 1, h = lane >> 5, m = lane & 31;
    f32x16 qa[8];
#pragma unroll
    for (int a = 0; a < 8; ++a) qa[a] = (f32x16)(0.f);
    const unsigned wbase = lds0 + (8 * h + qq) * 1024 + hh * 512 + (((2 * (g & 1) + (pp >> 1)) ^ (qq << 2)) << 4) + (pp & 1) * 8;
    const unsigned xbase = lds0 + XIMG + (32 * mh + m) * 128 + ((h ^ ((m >> 1) & 7)) << 4);
    bf16x8 wfA[8], wfB[8], xa, xb, xo, xc = (bf16x8)(0), xd = (bf16x8)(0);
#pragma unroll
    for (int a = 0; a < 8; ++a) wfB[a] = (bf16x8)(0);
    xo = (bf16x8)(0);
    int sl = SS;
    unsigned tch = 0, tch2 = 0;
    if (code_len < 65536u) {
      unsigned o = (w * 64 + lane) * 128; o = o < code_len ? o : 0;
      asm volatile("global_load_dword %0, %1, %2" : "=v"(tch) : "v"(o), "s"(pc0) : "memory");
      o = 32768 + (w * 64 + lane) * 128; o = o < code_len ? o : 0;
      asm volatile("global_load_dword %0, %1, %2" : "=v"(tch2) : "v"(o), "s"(pc0) : "memory");
    }
    for (int i = 0; i < 14; ++i) {
      CBAR();
      unsigned p = wbase + sl; OPAQUE(p);
      sl = sl == 2 * SS ? 0 : sl + SS;
      const unsigned xp = (xbase + (i >> 1) * 8192) ^ (unsigned)((i & 1) << 6);
      if (i < 13) { xa = ldsb128(xp); xb = ldsb128(xp ^ 32); } else { xa = xc; xb = xd; }
      if (i == 12) { xc = ldsb128(xp ^ 64); xd = ldsb128(xp ^ 96); }
      SBAR();
#pragma unroll
      for (int a = 0; a < 8; ++a) {
        const unsigned q = (p ^ (unsigned)((a & 3) << 6)) + (a >> 2) * 256; wfA[a] = cat8(ldstr(q), ldstr(q + 4096));
        qa[a] = MFMA32(wfB[a], xo, qa[a]);
        SBAR();
      }
#pragma unroll
      for (int a = 0; a < 8; ++a) {
        if (a < 4) {
#pragma unroll
          for (int u = 0; u < 2; ++u) { const int a2 = 2 * a + u; const unsigned q = (p ^ (unsigned)((a2 & 3) << 6)) + (a2 >> 2) * 256; wfB[a2] = cat8(ldstr(q + 16384), ldstr(q + 16384 + 4096)); }
        }
        qa[a] = MFMA32(wfA[a], xa, qa[a]);
        SBAR();
      }
      xo = xb;
    }
    asm volatile("s_waitcnt vmcnt(0)" : "+v"(tch), "+v"(tch2) :: "memory");
    const unsigned qw = lds0 + QST + (hh * 2 + mh) * 16384 + (m >> 4) * 8192 + (m & 15) * 16 + h * 8;
#pragma unroll
    for (int nb = 0; nb < 8; ++nb) {
      qa[nb] = MFMA32(wfB[nb], xo, qa[nb]);
#pragma unroll
      for (int a = 0; a < 4; ++a)
        *(LAS u32x2*)(uintptr_t)(qw + nb * 1024 + a * 256) =
            u32x2{cvt2(qa[nb][4 * a] * CEXP, qa[nb][4 * a + 1] * CEXP), cvt2(qa[nb][4 * a + 2] * CEXP, qa[nb][4 * a + 3] * CEXP)};
    }
  }
  CBAR();
  bf16x8 qf[2][8];
  {
    const unsigned qr = lds0 + QST + rg * 16384 + lane_id() * 16;
#pragma unroll
    for (int qb = 0; qb < 2; ++qb)
#pragma unroll
      for (int s = 0; s < 8; ++s) qf[qb][s] = ldsb128(qr + (qb * 8 + s) * 1024);
  }
  if (attempt == STAMP_ATT) STAMP(1);

    tk0 = (unsigned)__builtin_amdgcn_s_memrealtime();
    f32x4 sa[2][2];
    f32x4 nm[2];
    float tmax[2] = {0.f, 0.f}, lsum[2] = {0.f, 0.f};
    auto kmm = [&](auto tt, int f, bf16x8 a) {
      constexpr int t = decltype(tt)::value;
      if (f >= 2) mm2(sa[f & 1][0], sa[f & 1][1], a, qf[0][f >> 1], qf[1][f >> 1]);
      else if (t == 0) mm2z(sa[f][0], sa[f][1], a, qf[0][0], qf[1][0]);
      else mm2c(sa[f][0], sa[f][1], a, qf[0][0], qf[1][0], nm[0], nm[1]);
    };
    auto smax = [&](int qb) {
      return fmaxf(fmaxf(fmaxf(sa[0][qb][0], sa[0][qb][1]), fmaxf(sa[0][qb][2], sa[0][qb][3])), fmaxf(fmaxf(sa[1][qb][0], sa[1][qb][1]), fmaxf(sa[1][qb][2], sa[1][qb][3])));
    };
    auto soft = [&](auto tt) {
      constexpr int t = decltype(tt)::value;
      MFMA_DRAIN();
#pragma unroll
      for (int qb = 0; qb < 2; ++qb) {
        if (t == 0) {
          mref[qb] = fmaxf(mref[qb], xmax(smax(qb)));
          nm[qb] = (f32x4)(-mref[qb]); OPAQUE(nm[qb]);
          sa[0][qb] += nm[qb]; sa[1][qb] += nm[qb];
        } else if (attempt == 1) tmax[qb] = fmaxf(tmax[qb], smax(qb));
        float e[8];
#pragma unroll
        for (int r = 0; r < 8; ++r) e[r] = __builtin_amdgcn_exp2f(sa[r >> 2][qb][r & 3]);
        lsum[qb] += ((e[0] + e[1]) + (e[2] + e[3])) + ((e[4] + e[5]) + (e[6] + e[7])); OPAQUE(lsum[qb]);
        pst[t][qb] = u32x4{cvt2(e[0], e[1]), cvt2(e[2], e[3]), cvt2(e[4], e[5]), cvt2(e[6], e[7])};
        OPAQUE(pst[t][qb]);
      }
    };
    auto kstep = [&](auto tt) {
      constexpr int t = decltype(tt)::value;
      const int ln = lane_id();
      unsigned kb0 = lds0 + (t & 3) * SS + hf * 16384 + (ln & 15) * 512 + (((ln >> 4) ^ (ln & 15)) << 4); OPAQUE(kb0);
      if (t > 1) PACE(t - 1);
      if (t == 0) wbar<8>(); else wbar<4>();
      if (hf == 1) issue(t + 2);
      ring_step<16, NBK, 2>(carry,
          [&](int f) { return ldsb128((kb0 ^ (unsigned)(((f >> 1) & 3) << 6)) + (f >> 3) * 256 + (f & 1) * 8192); },
          [&](int f, bf16x8 a) { kmm(IC<t>{}, f, a); },
          [&](int r, bf16x8 a) { if constexpr (t > 0) kmm(IC<t - 1>{}, 16 - RDEF + r, a); },
          [&]() { if constexpr (t > 0) soft(IC<t - 1>{}); }, nohook);
      if (hf == 0) issue(t + 2);
    };
    kstep(IC<0>{}); kstep(IC<1>{}); kstep(IC<2>{}); kstep(IC<3>{}); kstep(IC<4>{}); kstep(IC<5>{}); kstep(IC<6>{}); kstep(IC<7>{});
    kstep(IC<8>{}); kstep(IC<9>{}); kstep(IC<10>{}); kstep(IC<11>{}); kstep(IC<12>{}); kstep(IC<13>{}); kstep(IC<14>{}); kstep(IC<15>{});
    if (attempt == STAMP_ATT) STAMP(2);
    {
      const unsigned vb = vaddr(0); PACE(15); wbar<4>(); if (hf == 1) { issue_vw(18); issue_vw(19); }
      ring_step<16, NBV, 2>(carry, [&](int f) { return vrd(IC<0>{}, vb, f); }, [&](int f, bf16x8 a) { vmm(IC<0>{}, f, a); },
                            [&](int r, bf16x8 a) { kmm(IC<15>{}, 16 - RDEF + r, a); },
                            [&]() {
                              soft(IC<15>{});
                              const int ln = lane_id();
                              lsum[0] = xsum(lsum[0]); lsum[1] = xsum(lsum[1]);
                              const bool bad = !(lsum[0] <= LMAX) || !(lsum[1] <= LMAX);
                              const float anybad = __any(bad) ? 1.f : 0.f;
                              if (ln < 16) *(LAS f32x4*)(uintptr_t)(lds0 + MISC + w * 256 + ln * 16) = f32x4{mref[0], lsum[0], mref[1], lsum[1]};
                              if (ln == 0) *(LAS float*)(uintptr_t)(lds0 + MISC + 2048 + w * 4) = anybad;
                            }, nohook);
      if (hf == 0) { issue_vw(18); issue_vw(19); }
    }
    float fl;
    {
      const unsigned vb = vaddr(1); PACE(16); wbar<8>(); if (hf == 1) issue_vw(20);
      ring_step<16, NBV, 2>(carry, [&](int f) { return vrd(IC<1>{}, vb, f); }, [&](int f, bf16x8 a) { vmm(IC<1>{}, f, a); },
                         [&](int r, bf16x8 a) { vmm(IC<0>{}, 16 - RDEF + r, a); },
                         [&]() {
                           const int ln = lane_id();
                           const f32x4 o = *(const LAS f32x4*)(uintptr_t)(lds0 + MISC + (w ^ 4) * 256 + (ln & 15) * 16);
                           const f32x4 f0 = *(const LAS f32x4*)(uintptr_t)(lds0 + MISC + 2048), f1 = *(const LAS f32x4*)(uintptr_t)(lds0 + MISC + 2048 + 16);
                           fl = (f0[0] + f0[1]) + (f0[2] + f0[3]) + (f1[0] + f1[1]) + (f1[2] + f1[3]); OPAQUE(fl);
                           const bool odd = (ln >> 4) & 1;
                           float l0 = lsum[0], l1 = lsum[1], m0 = mref[0], m1 = mref[1], o0_ = o[0], o1_ = o[1], o2_ = o[2], o3_ = o[3];
                           OPAQUE(l0); OPAQUE(l1); OPAQUE(m0); OPAQUE(m1); OPAQUE(o0_); OPAQUE(o1_); OPAQUE(o2_); OPAQUE(o3_);
                           const float ms = odd ? m1 : m0, ls = odd ? l1 : l0, mo = odd ? o2_ : o0_, lo = odd ? o3_ : o1_;
                           const float m = fmaxf(ms, mo), ea = __builtin_amdgcn_exp2f(ms - m), eb = __builtin_amdgcn_exp2f(mo - m);
                           cpk = ea / (ls * ea + lo * eb); OPAQUE(cpk);
                         }, nohook);
      if (hf == 0) issue_vw(20);
    }
    if (attempt == 2 || (attempt == 0 && __builtin_amdgcn_readfirstlane(__float_as_uint(fl)) == 0u)) break;
    mref[0] += xmax(tmax[0]); mref[1] += xmax(tmax[1]);
    wbar<0>();
  }
  {
    const unsigned vb = vaddr(2); PACE(17); wbar<8>(); if (hf == 1) issue_vw(21);
    ring_step<16, NBV, 2>(carry, [&](int f) { return vrd(IC<2>{}, vb, f); }, [&](int f, bf16x8 a) { vmm(IC<2>{}, f, a); },
                          [&](int r, bf16x8 a) { vmm(IC<1>{}, 16 - RDEF + r, a); }, nop, nohook);
    if (hf == 0) issue_vw(21);
  }
  {
    const unsigned vb = vaddr(3); PACE(18); wbar<8>(); if (hf == 1) issue_vw(22);
    ring_step<16, NBV, 2>(carry, [&](int f) { return vrd(IC<3>{}, vb, f); }, [&](int f, bf16x8 a) { vmm(IC<3>{}, f, a); },
                          [&](int r, bf16x8 a) { vmm(IC<2>{}, 16 - RDEF + r, a); }, nop, nohook);
  }

  const int qh = w >> 2, nq = w & 3;
  f32x4 pa[7][2];
#pragma unroll
  for (int i = 0; i < 7; ++i) { pa[i][0] = (f32x4)(0.f); pa[i][1] = (f32x4)(0.f); }
  bf16x8 o0 = (bf16x8)(0), o1 = (bf16x8)(0);
  auto oload = [&](int idx) {
    const unsigned oad = lds0 + MISC + ((((idx >> 1) * 2 + qh) + 4 * ((idx & 1) ^ 1)) * 4096) + lane_id() * 16;
    o0 = ldsb128(oad); o1 = ldsb128(oad + 1024);
  };
  auto wmm = [&](int i, bf16x8 a) { mm2(pa[i][0], pa[i][1], a, o0, o1); };
  float c0, c1;
  auto comb_write = [&](int k) {
    if (k == 0) {
      float cc = cpk; OPAQUE(cc);
      const auto cr = __builtin_amdgcn_permlane16_swap(__float_as_uint(cc), __float_as_uint(cc), false, false);
      c0 = __uint_as_float(cr[0]); c1 = __uint_as_float(cr[1]);
    }
    if (k < 2) {
      const unsigned ad = lds0 + MISC + w * 4096 + k * 2048 + lane_id() * 16;
      *(LAS f32x4*)(uintptr_t)(ad) = oa[2 + k][0] * c0; *(LAS f32x4*)(uintptr_t)(ad + 1024) = oa[2 + k][1] * c1;
    } else if (k < 4) { oa[k - 2][0] *= c0; oa[k - 2][1] *= c1; PIN2(oa[k - 2][0], oa[k - 2][1]); }
  };
  f32x4 ct0, ct1; u32x4 cf0;
  auto comb_rd = [&](int qb) {
    const unsigned pad = lds0 + MISC + (w ^ 4) * 4096 + qb * 1024 + lane_id() * 16;
    ct0 = *(const LAS f32x4*)(uintptr_t)(pad); ct1 = *(const LAS f32x4*)(uintptr_t)(pad + 2048);
  };
  auto comb_fin = [&](int qb) {
    const f32x4 a = ct0 + oa[0][qb], c = ct1 + oa[1][qb];
    *(LAS u32x4*)(uintptr_t)(lds0 + MISC + (w ^ 4) * 4096 + qb * 1024 + lane_id() * 16) = u32x4{cvt2(a[0], a[1]), cvt2(a[2], a[3]), cvt2(c[0], c[1]), cvt2(c[2], c[3])};
  };
  auto vstep = [&](int slot, int pn, auto cc) {
    constexpr int c = decltype(cc)::value;
    const unsigned vb = vaddr(slot);
    wbar<8>(); TS(c == 0 ? 2 : 1);
    if (hf == 1) issue_vw(pn);
    ring_step<16, (c == 0 ? NBV - 1 : NBV), 2>(carry, [&](int f) { return vrd(cc, vb, f); }, [&](int f, bf16x8 a) { vmm(cc, f, a); },
                          [&](int r, bf16x8 a) { wmm(7 - RDEF + r, a); }, [&]() { if (c == 0) comb_rd(0); },
                          [&](int f) { if (c == 0 && f == 2) { comb_fin(0); comb_rd(1); } if (c == 0 && f == 6) comb_fin(1); });
  };
  auto wstep = [&](int slot, auto pre, auto prev, auto mid, auto hook) {
    const int ln = lane_id(), rl = ((ln >> 4) << 2) | ((ln >> 2) & 3);
    unsigned wb = lds0 + slot * SS + rl * 128 + (((2 * nq + ((ln >> 1) & 1)) ^ (rl & 6)) << 4) + (ln & 1) * 8;
    OPAQUE(wb);
    pre();
    ring_step<7, NBW, NBW - RDEF>(carry, [&](int i) { return cat8(ldstr(wb + i * 4096), ldstr(wb + i * 4096 + 2048)); }, wmm, prev, mid, hook);
  };
#pragma unroll
  for (int r = 0; r < RDEF; ++r) { vmm(IC<3>{}, 16 - RDEF + r, carry[r]); carry[r] = (bf16x8)(0); }
  if (hf == 0) issue_vw(22);
  MFMA_DRAIN();
  comb_write(0); comb_write(1); comb_write(2); comb_write(3);
#pragma unroll 1
  for (int p = 20; p < 44; p += 8) {
    vstep(0, p + 3, IC<0>{}); if (hf == 0) issue_vw(p + 3);
    wstep(1, [&]() { wbar<8>(); TS(3); if (hf == 1) issue_vw(p + 4); oload(0); }, [&](int r, bf16x8 a) { vmm(IC<0>{}, 16 - RDEF + r, a); }, nop, nohook); if (hf == 0) issue_vw(p + 4);
    vstep(2, p + 5, IC<1>{}); if (hf == 0) issue_vw(p + 5);
    wstep(3, [&]() { wbar<8>(); TS(0); if (hf == 1) issue_vw(p + 6); oload(1); }, [&](int r, bf16x8 a) { vmm(IC<1>{}, 16 - RDEF + r, a); }, nop, nohook); if (hf == 0) issue_vw(p + 6);
    vstep(0, p + 7, IC<2>{}); if (hf == 0) issue_vw(p + 7);
    wstep(1, [&]() { wbar<8>(); TS(0); if (hf == 1) issue_vw(p + 8); oload(2); }, [&](int r, bf16x8 a) { vmm(IC<2>{}, 16 - RDEF + r, a); }, nop, nohook); if (hf == 0) issue_vw(p + 8);
    vstep(2, p + 9, IC<3>{}); oload(3); if (hf == 0) issue_vw(p + 9);
    wstep(3, [&]() { wbar<8>(); TS(0); if (hf == 1) issue_vw(p + 10); }, [&](int r, bf16x8 a) { vmm(IC<3>{}, 16 - RDEF + r, a); }, nop,
          [&](int f) { if (f >= 1) comb_write(f - 1); }); if (hf == 0) issue_vw(p + 10);
  }
  STAMP(3);
  CBAR();
  comb_rd(0);
#pragma unroll
  for (int r = 0; r < RDEF; ++r) wmm(7 - RDEF + r, carry[r]);
  comb_fin(0); comb_rd(1); issue_vw(47); comb_fin(1);
  unsigned long long* ctl = (unsigned long long*)(ws + (size_t)grp * 2048);
  const __amdgpu_buffer_rsrc_t wsr = __builtin_amdgcn_make_buffer_rsrc(ws, 0, 0x7fffffff, 0x00020000);
  const int lane = lane_id(), g = lane >> 4, li = lane & 15;
  unsigned long long ep;
  {
    const int rl = (g << 2) | ((lane >> 2) & 3);
    unsigned wb = lds0 + rl * 128 + (((2 * nq + ((lane >> 1) & 1)) ^ (rl & 6)) << 4) + (lane & 1) * 8, wb2 = wb + 2 * SS;
    PIN2(wb, wb2);
    wbar<0>();
    ep = __hip_atomic_load(ctl, __ATOMIC_RELAXED, __HIP_MEMORY_SCOPE_AGENT);
    bf16x8 of[4][2];
#pragma unroll
    for (int k = 0; k < 4; ++k) { oload(k); of[k][0] = o0; of[k][1] = o1; }
    auto st = [&](int i) {
      u32x4 f = {cvt2(pa[i][0][0], pa[i][0][1]), cvt2(pa[i][0][2], pa[i][0][3]), cvt2(pa[i][1][0], pa[i][1][1]), cvt2(pa[i][1][2], pa[i][1][3])};
      const unsigned off = (unsigned)WS_PART + (((unsigned)(grp * 56 + w * 7 + i) * 8 + kvh) << 10) + lane * 16;
      __builtin_amdgcn_raw_buffer_store_b128(f, wsr, off, 0, 16);
    };
    ring_step<28 + RDEF, 8, 6>(carry,
        [&](int f) { const int i = (f >> 2) % 7, k = f & 3; const unsigned a = ((k & 2) ? wb2 : wb) + (k & 1) * SS + i * 4096; return cat8(ldstr(a), ldstr(a + 2048)); },
        [&](int f, bf16x8 a) { mm2(pa[f >> 2][0], pa[f >> 2][1], a, of[f & 3][0], of[f & 3][1]); },
        [&](int, bf16x8) {}, nop,
        [&](int f) { if ((f & 3) == 2 && f >= 4) st((f >> 2) - 1); });
    MFMA_DRAIN();
    st(6);
  }
  const unsigned long long tag = ep * 0x9E3779B97F4A7C15ull + 0x632BE59BD9B4E019ull;
  STAMP(4);
  asm volatile("gqa_tail_label:");
  wbar<0>();
  STAMP(5);
  if (w == 0 && lane == 0) __hip_atomic_store(ctl + 16 * (kvh + 1), tag, __ATOMIC_RELAXED, __HIP_MEMORY_SCOPE_AGENT);
  if (w < 7) {
    for (unsigned spin = 0; spin < (1u << 22); ++spin) {
      unsigned long long f = tag;
      if (lane < 8) f = __hip_atomic_load(ctl + 16 * (lane + 1), __ATOMIC_RELAXED, __HIP_MEMORY_SCOPE_AGENT);
      if (__all(f == tag)) break;
      __builtin_amdgcn_s_sleep(2);
    }
    __builtin_amdgcn_fence(__ATOMIC_ACQUIRE, "wavefront");
    STAMP(6);
    const unsigned off = (unsigned)WS_PART + ((unsigned)(grp * 56 + kvh * 7 + w) << 13) + lane * 16;
    u32x4 v[8];
#pragma unroll
    for (int s = 0; s < 8; ++s) v[s] = __builtin_amdgcn_raw_buffer_load_b128(wsr, off + s * 1024, 0, 16);
    float acc[8];
#pragma unroll
    for (int e = 0; e < 8; ++e) acc[e] = 0.f;
#pragma unroll
    for (int s = 0; s < 8; ++s)
#pragma unroll
      for (int e = 0; e < 4; ++e) { acc[2 * e] += __uint_as_float(v[s][e] << 16); acc[2 * e + 1] += __uint_as_float(v[s][e] & 0xffff0000u); }
    const int q = 32 * (kvh >> 2) + li, n = 64 * w + 16 * (kvh & 3) + 4 * g;
    char* o = out + ((size_t)(b * SQ + q) * (2 * DM) + br * DM + n) * 2;
    *(u32x2*)o = u32x2{cvt2(acc[0], acc[1]), cvt2(acc[2], acc[3])};
    *(u32x2*)(o + 16 * 2 * DM * 2) = u32x2{cvt2(acc[4], acc[5]), cvt2(acc[6], acc[7])};
    STAMP(7);
    if (kvh == 0 && w == 0 && lane == 0) __hip_atomic_store(ctl, tag, __ATOMIC_RELAXED, __HIP_MEMORY_SCOPE_AGENT);
  }
}

extern "C" void kernel_launch(void* const* d_in, const int* in_sizes, int n_in, void* d_out, int out_size, void* d_ws, size_t ws_size,
                              hipStream_t stream) {
  (void)in_sizes; (void)n_in; (void)out_size; (void)ws_size;
  (void)hipFuncSetAttribute((const void*)gqa_fused_kernel, hipFuncAttributeMaxDynamicSharedMemorySize, LDS_BYTES);
  gqa_fused_kernel<<<256, 512, LDS_BYTES, stream>>>(
      (const char*)d_in[0], (const char*)d_in[1], (const char*)d_in[2], (const char*)d_in[3], (const char*)d_in[4],
      (const char*)d_in[5], (const char*)d_in[6], (const char*)d_in[7], (const char*)d_in[8], (const char*)d_in[9],
      (char*)d_out, (char*)d_ws);
}
```

```cpp
#include <hip/hip_runtime.h>
#include <hip/hip_bf16.h>
#include <stdint.h>
#include <type_traits>

typedef __attribute__((ext_vector_type(8))) short bf16x8;
typedef __attribute__((ext_vector_type(4))) short s16x4;
typedef __attribute__((ext_vector_type(4))) float f32x4;
typedef __attribute__((ext_vector_type(16))) float f32x16;
typedef __attribute__((ext_vector_type(2))) float f32x2;
typedef __attribute__((ext_vector_type(2))) __bf16 bf16x2_t;
typedef __attribute__((ext_vector_type(4))) unsigned u32x4;
typedef __attribute__((ext_vector_type(2))) unsigned u32x2;
#define LAS __attribute__((address_space(3)))
typedef LAS char* lds_ptr;

constexpr int SQ = 64, DM = 448, HKV = 8, SK = 1024, DH = 256;
constexpr int SS = 32768;
constexpr int MISC = 4 * SS;
constexpr int LDS_BYTES = 5 * SS;
constexpr int XIMG = 3 * SS;
constexpr int QST = 3 * SS;
constexpr float CEXP = 0.0625f * 1.4426950408889634f;
#define LMAX 1.0e18f
#define STAMP_ATT 0
#define NBK 5
#define NBV 6
#define NBW 4
constexpr int RDEF = 2;

constexpr size_t WS_PART = 65536;

#define MFMA16(a, b, c) __builtin_amdgcn_mfma_f32_16x16x32_bf16((a), (b), (c), 0, 0, 0)
#define MFMA32(a, b, c) __builtin_amdgcn_mfma_f32_32x32x16_bf16((a), (b), (c), 0, 0, 0)
#define CBAR() asm volatile("s_waitcnt lgkmcnt(0)\n\ts_barrier" ::: "memory")
#define SBAR() __builtin_amdgcn_sched_barrier(0)
#define OPAQUE(x) asm volatile("" : "+v"(x))
#define PIN2(x, y) asm volatile("" : "+v"(x), "+v"(y))
#define SOPAQUE(x) asm volatile("" : "+s"(x))
#define DI __device__ __forceinline__
template <int N> DI void wbar() { asm volatile("s_waitcnt vmcnt(%0) lgkmcnt(0)\n\ts_barrier" ::"i"(N) : "memory"); }
DI void bar() { asm volatile("s_barrier" ::: "memory"); }
template <int N> using IC = std::integral_constant<int, N>;

DI unsigned cvt2(float a, float b) { f32x2 v = {a, b}; return __builtin_bit_cast(unsigned, __builtin_convertvector(v, bf16x2_t)); }
DI bf16x8 ldsb128(unsigned a) { return *(const LAS bf16x8*)(uintptr_t)a; }
DI s16x4 ldstr(unsigned a) { return __builtin_amdgcn_ds_read_tr16_b64_v4i16((LAS s16x4*)(uintptr_t)a); }
DI bf16x8 cat8(s16x4 a, s16x4 b) { return __builtin_shufflevector(a, b, 0, 1, 2, 3, 4, 5, 6, 7); }
DI int lane_id() { int l; asm volatile("v_mbcnt_lo_u32_b32 %0, -1, 0\n\tv_mbcnt_hi_u32_b32 %0, -1, %0" : "=v"(l)); return l; }
DI float xsum(float v) {
  { auto r = __builtin_amdgcn_permlane16_swap(__float_as_uint(v), __float_as_uint(v), false, false); v = __uint_as_float(r[0]) + __uint_as_float(r[1]); }
  { auto r = __builtin_amdgcn_permlane32_swap(__float_as_uint(v), __float_as_uint(v), false, false); v = __uint_as_float(r[0]) + __uint_as_float(r[1]); }
  return v;
}
DI float xmax(float v) {
  { auto r = __builtin_amdgcn_permlane16_swap(__float_as_uint(v), __float_as_uint(v), false, false); v = fmaxf(__uint_as_float(r[0]), __uint_as_float(r[1])); }
  { auto r = __builtin_amdgcn_permlane32_swap(__float_as_uint(v), __float_as_uint(v), false, false); v = fmaxf(__uint_as_float(r[0]), __uint_as_float(r[1])); }
  return v;
}

DI void mm2(f32x4& c0, f32x4& c1, bf16x8 a, bf16x8 b0, bf16x8 b1) {
  asm volatile("v_mfma_f32_16x16x32_bf16 %0, %2, %3, %0\n\tv_mfma_f32_16x16x32_bf16 %1, %2, %4, %1" : "+v"(c0), "+v"(c1) : "v"(a), "v"(b0), "v"(b1));
}
DI void mm2z(f32x4& c0, f32x4& c1, bf16x8 a, bf16x8 b0, bf16x8 b1) {
  asm volatile("v_mfma_f32_16x16x32_bf16 %0, %2, %3, 0\n\tv_mfma_f32_16x16x32_bf16 %1, %2, %4, 0" : "=&v"(c0), "=&v"(c1) : "v"(a), "v"(b0), "v"(b1));
}
DI void mm2c(f32x4& c0, f32x4& c1, bf16x8 a, bf16x8 b0, bf16x8 b1, f32x4 i0, f32x4 i1) {
  asm volatile("v_mfma_f32_16x16x32_bf16 %0, %2, %3, %5\n\tv_mfma_f32_16x16x32_bf16 %1, %2, %4, %6" : "=&v"(c0), "=&v"(c1) : "v"(a), "v"(b0), "v"(b1), "v"(i0), "v"(i1));
}
#define MFMA_DRAIN() do { asm volatile("s_nop 7\n\ts_nop 1" ::: "memory"); SBAR(); } while (0)

DI void dma4(unsigned lds, const void* sbase, unsigned v0, unsigned v1, unsigned v2, unsigned v3) {
  unsigned keep;
  asm volatile("s_mov_b32 %0, m0\n\ts_mov_b32 m0, %1\n\ts_nop 3\n\t"
               "global_load_lds_dwordx4 %2, %6\n\tglobal_load_lds_dwordx4 %3, %6 offset:1024\n\t"
               "global_load_lds_dwordx4 %4, %6 offset:2048\n\tglobal_load_lds_dwordx4 %5, %6 offset:3072\n\ts_mov_b32 m0, %0"
               : "=&s"(keep) : "s"(lds), "v"(v0), "v"(v1), "v"(v2), "v"(v3), "s"(sbase) : "memory");
}
DI void dma2(unsigned lds, const void* sbase, unsigned v0, unsigned v1) {
  unsigned keep;
  asm volatile("s_mov_b32 %0, m0\n\ts_mov_b32 m0, %1\n\ts_nop 3\n\t"
               "global_load_lds_dwordx4 %2, %4\n\tglobal_load_lds_dwordx4 %3, %4 offset:1024\n\ts_mov_b32 m0, %0"
               : "=&s"(keep) : "s"(lds), "v"(v0), "v"(v1), "s"(sbase) : "memory");
}
DI void dma4s(unsigned lds, const char* s, size_t gstride, unsigned v) {
  unsigned keep;
  asm volatile("s_mov_b32 %0, m0\n\ts_mov_b32 m0, %1\n\ts_nop 3\n\tglobal_load_lds_dwordx4 %2, %3 nt\n\t"
               "s_add_u32 m0, m0, 0x2000\n\ts_nop 0\n\tglobal_load_lds_dwordx4 %2, %4 nt\n\t"
               "s_add_u32 m0, m0, 0x2000\n\ts_nop 0\n\tglobal_load_lds_dwordx4 %2, %5 nt\n\t"
               "s_add_u32 m0, m0, 0x2000\n\ts_nop 0\n\tglobal_load_lds_dwordx4 %2, %6 nt\n\ts_mov_b32 m0, %0"
               : "=&s"(keep) : "s"(lds), "v"(v), "s"(s), "s"(s + gstride), "s"(s + 2 * gstride), "s"(s + 3 * gstride) : "memory", "scc");
}

DI void dma4s_keep(unsigned lds, const char* s, size_t gstride, unsigned v) {
  unsigned keep;
  asm volatile("s_mov_b32 %0, m0\n\ts_mov_b32 m0, %1\n\ts_nop 3\n\tglobal_load_lds_dwordx4 %2, %3\n\t"
               "s_add_u32 m0, m0, 0x400\n\ts_nop 0\n\tglobal_load_lds_dwordx4 %2, %4\n\t"
               "s_add_u32 m0, m0, 0x400\n\ts_nop 0\n\tglobal_load_lds_dwordx4 %2, %5\n\t"
               "s_add_u32 m0, m0, 0x400\n\ts_nop 0\n\tglobal_load_lds_dwordx4 %2, %6\n\ts_mov_b32 m0, %0"
               : "=&s"(keep) : "s"(lds), "v"(v), "s"(s), "s"(s + gstride), "s"(s + 2 * gstride), "s"(s + 3 * gstride) : "memory", "scc");
}

template <int NF, int NB, int NI, class RD, class MM, class PREV, class MID, class HOOK>
DI void ring_step(bf16x8 (&carry)[RDEF], RD rd, MM mm, PREV prev, MID mid, HOOK hook) {
  static_assert(NI + RDEF <= NB, "");
  bf16x8 fb[NB];
#pragma unroll
  for (int f = 0; f < NI; ++f) fb[f] = rd(f);
  SBAR();
#pragma unroll
  for (int r = 0; r < RDEF; ++r) { prev(r, carry[r]); SBAR(); fb[NI + r] = rd(NI + r); SBAR(); }
  mid();
  SBAR();
#pragma unroll
  for (int f = NI + RDEF; f < NB; ++f) fb[f] = rd(f);
  SBAR();
#pragma unroll
  for (int f = 0; f < NF - RDEF; ++f) {
    mm(f, fb[f % NB]); SBAR();
    if (f + NB < NF) { fb[f % NB] = rd(f + NB); SBAR(); }
    hook(f); SBAR();
  }
#pragma unroll
  for (int r = 0; r < RDEF; ++r) carry[r] = fb[(NF - RDEF + r) % NB];
}

#define TAU 0
#define PACE(n) do { if (TAU && w == 7) { while ((unsigned)__builtin_amdgcn_s_memrealtime() - tk0 < (unsigned)((n) * TAU)) __builtin_amdgcn_s_sleep(1); } } while (0)
#define TS(k) do {} while (0)
#define STAMP(i) do {} while (0)

__global__ __launch_bounds__(512, 2) void gqa_fused_kernel(
    const char* __restrict__ xA, const char* __restrict__ kA, const char* __restrict__ vA, const char* __restrict__ wqA, const char* __restrict__ woA,
    const char* __restrict__ xB, const char* __restrict__ kB, const char* __restrict__ vB, const char* __restrict__ wqB, const char* __restrict__ woB,
    char* __restrict__ out, char* __restrict__ ws) {
  extern __shared__ __attribute__((aligned(16))) char smem[];
  const unsigned lds0 = (unsigned)(uintptr_t)(lds_ptr)smem;
  int w = __builtin_amdgcn_readfirstlane(threadIdx.x >> 6);
  const int id = blockIdx.x, br = (id >> 2) & 1, kvh = 2 * (id & 3) + ((id >> 3) & 1), b = id >> 4, grp = br * 16 + b;
  int rot7 = b % 7;
  int rg = w & 3, hf = w >> 2;
  STAMP(0);
  unsigned long long pc0; unsigned code_len;
  asm volatile("s_getpc_b64 %0\ngqa_head_label:\n\ts_mov_b32 %1, gqa_tail_label-gqa_head_label" : "=s"(pc0), "=s"(code_len));
  const char* kg = (br ? kB : kA) + (size_t)(b * HKV + kvh) * (SK * DH * 2);
  const char* vg = (br ? vB : vA) + (size_t)(b * HKV + kvh) * (SK * DH * 2);
  const char* wog = (br ? woB : woA) + (size_t)kvh * 512 * (DM * 2);

  auto k_eighth = [&](int t, int e) {
    const int ln = lane_id(), hi = ln >> 5;
    const char* kb = kg; SOPAQUE(kb);
    dma4s(lds0 + (t & 3) * SS + e * 1024, kb + (size_t)t * SS + e * 1024, 8192, hi * 512 + (((ln & 31) ^ (2 * e + hi)) << 4));
  };

  int vrot = (id >> 3) & 3;
  auto issue_vw = [&](int p) {
    const unsigned dst = lds0 + (p & 3) * SS + w * 1024;
    const int ln = lane_id();
    const bool isv = p < 20 || (p < 44 && !(p & 1));
    if (isv) {
      const int n = p < 20 ? p - 16 : 4 + ((p - 20) >> 1);
      const int r8 = ln >> 3;
      const char* vb = vg; SOPAQUE(vb);
      dma4s(dst, vb + (size_t)((n & 3) * 256 + 8 * w) * 512 + (((n >> 2) + vrot) & 3) * 128, 64 * 512, r8 * 512 + (((ln & 7) ^ (r8 & 6)) << 4));
    } else {
      const int k = p < 44 ? (p - 20) >> 1 : p - 32, r8 = ln >> 3;
      const char* wb = wog; SOPAQUE(wb);
      dma4s_keep(lds0 + (p & 3) * SS + w * 4096, wb + (size_t)(((k >> 1) & 1) * 256 + (((k >> 2) + vrot) & 3) * 64 + (k & 1) * 32) * 896 + (w < 7 ? w : 6) * 128,
                 8 * 896, r8 * 896 + (((ln & 7) ^ (r8 & 6)) << 4));
    }
  };
  auto issue = [&](int p) { if (p < 16) k_eighth(p, w); else issue_vw(p); };

  u32x4 pst[16][2];
  f32x4 oa[4][2];
  bf16x8 carry[RDEF];
  float mref[2] = {-INFINITY, -INFINITY};
  unsigned tk0;
  float cpk;

  auto vaddr = [&](int slot) {
    const int ln = lane_id(), rl = ((ln >> 4) << 2) | ((ln >> 2) & 3);
    unsigned a = lds0 + slot * SS + hf * 4096 + rl * 128 + ((((ln >> 1) & 1) ^ (rl & 6) ^ (hf << 2)) << 4) + (ln & 1) * 8;
    OPAQUE(a);
    return a;
  };
  auto vu = [](int c, int f) { return c == 0 ? ((f >> 1) & 3) : (f >> 2); };
  auto vd = [](int c, int f) { return c == 0 ? ((f & 1) | ((f & 8) ? 0 : 2)) : (f & 3); };
  auto vrd = [&](auto cc, unsigned vb, int f) {
    constexpr int c = decltype(cc)::value;
    const unsigned a = (vb ^ (unsigned)(vd(c, f) << 5)) + vu(c, f) * 8192; return cat8(ldstr(a), ldstr(a + 2048));
  };
  auto vmm = [&](auto cc, int f, bf16x8 a) {
    constexpr int c = decltype(cc)::value;
    const int u = vu(c, f), db = vd(c, f);
    const bf16x8 b0 = __builtin_bit_cast(bf16x8, pst[4 * c + u][0]), b1 = __builtin_bit_cast(bf16x8, pst[4 * c + u][1]);
    if (c == 0 && u == 0) mm2z(oa[db][0], oa[db][1], a, b0, b1); else mm2(oa[db][0], oa[db][1], a, b0, b1);
  };
  auto nohook = [](int) {};
  auto nop = []() {};

  for (int attempt = 0;; ++attempt) {
  SOPAQUE(w); SOPAQUE(rot7); SOPAQUE(vrot); rg = w & 3; hf = w >> 2;
  if (__builtin_expect(w >= 4, 1)) {
    const int lw = w - 4, lane = lane_id();
    const char* xg = (br ? xB : xA) + (size_t)b * (SQ * DM * 2); SOPAQUE(xg);
    const char* wqg = (br ? wqB : wqA) + (size_t)kvh * 1024; SOPAQUE(wqg);
    unsigned wofs[4];
#pragma unroll
    for (int j = 0; j < 4; ++j) wofs[j] = j * (8192 - 1024) + ((lane ^ (j << 2)) << 4);
    int wsl = 1;
    auto wtile = [&](int i) {
      int G = (i >> 1) + rot7; G = G >= 7 ? G - 7 : G;
      const char* src = wqg + (size_t)(G * 64 + (i & 1) * 32 + 8 * lw) * 8192;
      const unsigned dst = lds0 + wsl * SS + lw * 8192;
      dma4(dst, src, wofs[0], wofs[1], wofs[2], wofs[3]);
      dma4(dst + 4096, src + 4 * 8192, wofs[0], wofs[1], wofs[2], wofs[3]);
      wsl = wsl == 2 ? 0 : wsl + 1;
    };
    const int xr = 16 * lw + (lane >> 3), xc = lane & 7;
    const unsigned x0 = xr * 896 + ((xc ^ ((xr >> 1) & 7)) << 4), x1 = (xr + 8) * 896 + ((xc ^ (((xr + 8) >> 1) & 7)) << 4) - 1024;
    auto xtile = [&](int Gi) { int G = Gi + rot7; G = G >= 7 ? G - 7 : G; dma2(lds0 + XIMG + Gi * 8192 + lw * 2048, xg + G * 128, x0, x1); };
    xtile(0); wtile(0); wtile(1);
    for (int i = 0; i < 14; ++i) {
      if (i == 1) wbar<20>(); else wbar<8>();
      if (i == 0) for (int Gi = 1; Gi < 7; ++Gi) xtile(Gi);
      if (i + 2 < 14) wtile(i + 2); else { k_eighth(i - 12, 2 * lw); k_eighth(i - 12, 2 * lw + 1); }
    }
  } else {
    const int lane = lane_id(), g = lane >> 4, qq = (lane >> 2) & 3, pp = lane & 3;
    const int mh = w & 1, hh = w >> 1, h = lane >> 5, m = lane & 31;
    f32x16 qa[8];
#pragma unroll
    for (int a = 0; a < 8; ++a) qa[a] = (f32x16)(0.f);
    const unsigned wbase = lds0 + (8 * h + qq) * 1024 + hh * 512 + (((2 * (g & 1) + (pp >> 1)) ^ (qq << 2)) << 4) + (pp & 1) * 8;
    const unsigned xbase = lds0 + XIMG + (32 * mh + m) * 128 + ((h ^ ((m >> 1) & 7)) << 4);
    bf16x8 wfA[8], wfB[8], xa, xb, xo, xc = (bf16x8)(0), xd = (bf16x8)(0);
#pragma unroll
    for (int a = 0; a < 8; ++a) wfB[a] = (bf16x8)(0);
    xo = (bf16x8)(0);
    int sl = SS;
    unsigned tch = 0, tch2 = 0;
    for (int i = 0; i < 14; ++i) {
      CBAR();
      if (i == 0 && code_len < 65536u) {
        unsigned o = (w * 64 + lane) * 128; o = o < code_len ? o : 0;
        asm volatile("global_load_dword %0, %1, %2" : "=v"(tch) : "v"(o), "s"(pc0) : "memory");
        o = 32768 + (w * 64 + lane) * 128; o = o < code_len ? o : 0;
        asm volatile("global_load_dword %0, %1, %2" : "=v"(tch2) : "v"(o), "s"(pc0) : "memory");
      }
      unsigned p = wbase + sl; OPAQUE(p);
      sl = sl == 2 * SS ? 0 : sl + SS;
      const unsigned xp = (xbase + (i >> 1) * 8192) ^ (unsigned)((i & 1) << 6);
      if (i < 13) { xa = ldsb128(xp); xb = ldsb128(xp ^ 32); } else { xa = xc; xb = xd; }
      if (i == 12) { xc = ldsb128(xp ^ 64); xd = ldsb128(xp ^ 96); }
      SBAR();
#pragma unroll
      for (int a = 0; a < 8; ++a) {
        const unsigned q = (p ^ (unsigned)((a & 3) << 6)) + (a >> 2) * 256; wfA[a] = cat8(ldstr(q), ldstr(q + 4096));
        qa[a] = MFMA32(wfB[a], xo, qa[a]);
        SBAR();
      }
#pragma unroll
      for (int a = 0; a < 8; ++a) {
        if (a < 4) {
#pragma unroll
          for (int u = 0; u < 2; ++u) { const int a2 = 2 * a + u; const unsigned q = (p ^ (unsigned)((a2 & 3) << 6)) + (a2 >> 2) * 256; wfB[a2] = cat8(ldstr(q + 16384), ldstr(q + 16384 + 4096)); }
        }
        qa[a] = MFMA32(wfA[a], xa, qa[a]);
        SBAR();
      }
      xo = xb;
    }
    asm volatile("s_waitcnt vmcnt(0)" : "+v"(tch), "+v"(tch2) :: "memory");
    const unsigned qw = lds0 + QST + (hh * 2 + mh) * 16384 + (m >> 4) * 8192 + (m & 15) * 16 + h * 8;
#pragma unroll
    for (int nb = 0; nb < 8; ++nb) {
      qa[nb] = MFMA32(wfB[nb], xo, qa[nb]);
#pragma unroll
      for (int a = 0; a < 4; ++a)
        *(LAS u32x2*)(uintptr_t)(qw + nb * 1024 + a * 256) =
            u32x2{cvt2(qa[nb][4 * a] * CEXP, qa[nb][4 * a + 1] * CEXP), cvt2(qa[nb][4 * a + 2] * CEXP, qa[nb][4 * a + 3] * CEXP)};
    }
  }
  CBAR();
  bf16x8 qf[2][8];
  {
    const unsigned qr = lds0 + QST + rg * 16384 + lane_id() * 16;
#pragma unroll
    for (int qb = 0; qb < 2; ++qb)
#pragma unroll
      for (int s = 0; s < 8; ++s) qf[qb][s] = ldsb128(qr + (qb * 8 + s) * 1024);
  }
  if (attempt == STAMP_ATT) STAMP(1);

    tk0 = (unsigned)__builtin_amdgcn_s_memrealtime();
    f32x4 sa[2][2];
    f32x4 nm[2];
    float tmax[2] = {0.f, 0.f}, lsum[2] = {0.f, 0.f};
    auto kmm = [&](auto tt, int f, bf16x8 a) {
      constexpr int t = decltype(tt)::value;
      if (f >= 2) mm2(sa[f & 1][0], sa[f & 1][1], a, qf[0][f >> 1], qf[1][f >> 1]);
      else if (t == 0) mm2z(sa[f][0], sa[f][1], a, qf[0][0], qf[1][0]);
      else mm2c(sa[f][0], sa[f][1], a, qf[0][0], qf[1][0], nm[0], nm[1]);
    };
    auto smax = [&](int qb) {
      return fmaxf(fmaxf(fmaxf(sa[0][qb][0], sa[0][qb][1]), fmaxf(sa[0][qb][2], sa[0][qb][3])), fmaxf(fmaxf(sa[1][qb][0], sa[1][qb][1]), fmaxf(sa[1][qb][2], sa[1][qb][3])));
    };
    auto soft = [&](auto tt) {
      constexpr int t = decltype(tt)::value;
      MFMA_DRAIN();
#pragma unroll
      for (int qb = 0; qb < 2; ++qb) {
        if (t == 0) {
          mref[qb] = fmaxf(mref[qb], xmax(smax(qb)));
          nm[qb] = (f32x4)(-mref[qb]); OPAQUE(nm[qb]);
          sa[0][qb] += nm[qb]; sa[1][qb] += nm[qb];
        } else if (attempt == 1) tmax[qb] = fmaxf(tmax[qb], smax(qb));
        float e[8];
#pragma unroll
        for (int r = 0; r < 8; ++r) e[r] = __builtin_amdgcn_exp2f(sa[r >> 2][qb][r & 3]);
        lsum[qb] += ((e[0] + e[1]) + (e[2] + e[3])) + ((e[4] + e[5]) + (e[6] + e[7])); OPAQUE(lsum[qb]);
        pst[t][qb] = u32x4{cvt2(e[0], e[1]), cvt2(e[2], e[3]), cvt2(e[4], e[5]), cvt2(e[6], e[7])};
        OPAQUE(pst[t][qb]);
      }
    };
    auto kstep = [&](auto tt) {
      constexpr int t = decltype(tt)::value;
      const int ln = lane_id();
      unsigned kb0 = lds0 + (t & 3) * SS + hf * 16384 + (ln & 15) * 512 + (((ln >> 4) ^ (ln & 15)) << 4); OPAQUE(kb0);
      if (t > 1) PACE(t - 1);
      if (t == 0) wbar<8>(); else wbar<4>();
      if (hf == 1) issue(t + 2);
      ring_step<16, NBK, 2>(carry,
          [&](int f) { return ldsb128((kb0 ^ (unsigned)(((f >> 1) & 3) << 6)) + (f >> 3) * 256 + (f & 1) * 8192); },
          [&](int f, bf16x8 a) { kmm(IC<t>{}, f, a); },
          [&](int r, bf16x8 a) { if constexpr (t > 0) kmm(IC<t - 1>{}, 16 - RDEF + r, a); },
          [&]() { if constexpr (t > 0) soft(IC<t - 1>{}); }, nohook);
      if (hf == 0) issue(t + 2);
    };
    kstep(IC<0>{}); kstep(IC<1>{}); kstep(IC<2>{}); kstep(IC<3>{}); kstep(IC<4>{}); kstep(IC<5>{}); kstep(IC<6>{}); kstep(IC<7>{});
    kstep(IC<8>{}); kstep(IC<9>{}); kstep(IC<10>{}); kstep(IC<11>{}); kstep(IC<12>{}); kstep(IC<13>{}); kstep(IC<14>{}); kstep(IC<15>{});
    if (attempt == STAMP_ATT) STAMP(2);
    {
      const unsigned vb = vaddr(0); PACE(15); wbar<4>(); if (hf == 1) { issue_vw(18); issue_vw(19); }
      ring_step<16, NBV, 2>(carry, [&](int f) { return vrd(IC<0>{}, vb, f); }, [&](int f, bf16x8 a) { vmm(IC<0>{}, f, a); },
                            [&](int r, bf16x8 a) { kmm(IC<15>{}, 16 - RDEF + r, a); },
                            [&]() {
                              soft(IC<15>{});
                              const int ln = lane_id();
                              lsum[0] = xsum(lsum[0]); lsum[1] = xsum(lsum[1]);
                              const bool bad = !(lsum[0] <= LMAX) || !(lsum[1] <= LMAX);
                              const float anybad = __any(bad) ? 1.f : 0.f;
                              if (ln < 16) *(LAS f32x4*)(uintptr_t)(lds0 + MISC + w * 256 + ln * 16) = f32x4{mref[0], lsum[0], mref[1], lsum[1]};
                              if (ln == 0) *(LAS float*)(uintptr_t)(lds0 + MISC + 2048 + w * 4) = anybad;
                            }, nohook);
      if (hf == 0) { issue_vw(18); issue_vw(19); }
    }
    float fl;
    {
      const unsigned vb = vaddr(1); PACE(16); wbar<8>(); if (hf == 1) issue_vw(20);
      ring_step<16, NBV, 2>(carry, [&](int f) { return vrd(IC<1>{}, vb, f); }, [&](int f, bf16x8 a) { vmm(IC<1>{}, f, a); },
                         [&](int r, bf16x8 a) { vmm(IC<0>{}, 16 - RDEF + r, a); },
                         [&]() {
                           const int ln = lane_id();
                           const f32x4 o = *(const LAS f32x4*)(uintptr_t)(lds0 + MISC + (w ^ 4) * 256 + (ln & 15) * 16);
                           const f32x4 f0 = *(const LAS f32x4*)(uintptr_t)(lds0 + MISC + 2048), f1 = *(const LAS f32x4*)(uintptr_t)(lds0 + MISC + 2048 + 16);
                           fl = (f0[0] + f0[1]) + (f0[2] + f0[3]) + (f1[0] + f1[1]) + (f1[2] + f1[3]); OPAQUE(fl);
                           const bool odd = (ln >> 4) & 1;
                           float l0 = lsum[0], l1 = lsum[1], m0 = mref[0], m1 = mref[1], o0_ = o[0], o1_ = o[1], o2_ = o[2], o3_ = o[3];
                           OPAQUE(l0); OPAQUE(l1); OPAQUE(m0); OPAQUE(m1); OPAQUE(o0_); OPAQUE(o1_); OPAQUE(o2_); OPAQUE(o3_);
                           const float ms = odd ? m1 : m0, ls = odd ? l1 : l0, mo = odd ? o2_ : o0_, lo = odd ? o3_ : o1_;
                           const float m = fmaxf(ms, mo), ea = __builtin_amdgcn_exp2f(ms - m), eb = __builtin_amdgcn_exp2f(mo - m);
                           cpk = ea / (ls * ea + lo * eb); OPAQUE(cpk);
                         }, nohook);
      if (hf == 0) issue_vw(20);
    }
    if (attempt == 2 || (attempt == 0 && __builtin_amdgcn_readfirstlane(__float_as_uint(fl)) == 0u)) break;
    mref[0] += xmax(tmax[0]); mref[1] += xmax(tmax[1]);
    wbar<0>();
  }
  {
    const unsigned vb = vaddr(2); PACE(17); wbar<8>(); if (hf == 1) issue_vw(21);
    ring_step<16, NBV, 2>(carry, [&](int f) { return vrd(IC<2>{}, vb, f); }, [&](int f, bf16x8 a) { vmm(IC<2>{}, f, a); },
                          [&](int r, bf16x8 a) { vmm(IC<1>{}, 16 - RDEF + r, a); }, nop, nohook);
    if (hf == 0) issue_vw(21);
  }
  {
    const unsigned vb = vaddr(3); PACE(18); wbar<8>(); if (hf == 1) issue_vw(22);
    ring_step<16, NBV, 2>(carry, [&](int f) { return vrd(IC<3>{}, vb, f); }, [&](int f, bf16x8 a) { vmm(IC<3>{}, f, a); },
                          [&](int r, bf16x8 a) { vmm(IC<2>{}, 16 - RDEF + r, a); }, nop, nohook);
  }

  const int qh = w >> 2, nq = w & 3;
  f32x4 pa[7][2];
#pragma unroll
  for (int i = 0; i < 7; ++i) { pa[i][0] = (f32x4)(0.f); pa[i][1] = (f32x4)(0.f); }
  bf16x8 o0 = (bf16x8)(0), o1 = (bf16x8)(0);
  auto oload = [&](int idx) {
    const unsigned oad = lds0 + MISC + ((((idx >> 1) * 2 + qh) + 4 * ((idx & 1) ^ 1)) * 4096) + lane_id() * 16;
    o0 = ldsb128(oad); o1 = ldsb128(oad + 1024);
  };
  auto wmm = [&](int i, bf16x8 a) { mm2(pa[i][0], pa[i][1], a, o0, o1); };
  float c0, c1;
  auto comb_write = [&](int k) {
    if (k == 0) {
      float cc = cpk; OPAQUE(cc);
      const auto cr = __builtin_amdgcn_permlane16_swap(__float_as_uint(cc), __float_as_uint(cc), false, false);
      c0 = __uint_as_float(cr[0]); c1 = __uint_as_float(cr[1]);
    }
    if (k < 2) {
      const unsigned ad = lds0 + MISC + w * 4096 + k * 2048 + lane_id() * 16;
      *(LAS f32x4*)(uintptr_t)(ad) = oa[2 + k][0] * c0; *(LAS f32x4*)(uintptr_t)(ad + 1024) = oa[2 + k][1] * c1;
    } else if (k < 4) { oa[k - 2][0] *= c0; oa[k - 2][1] *= c1; PIN2(oa[k - 2][0], oa[k - 2][1]); }
  };
  f32x4 ct0, ct1; u32x4 cf0;
  auto comb_rd = [&](int qb) {
    const unsigned pad = lds0 + MISC + (w ^ 4) * 4096 + qb * 1024 + lane_id() * 16;
    ct0 = *(const LAS f32x4*)(uintptr_t)(pad); ct1 = *(const LAS f32x4*)(uintptr_t)(pad + 2048);
  };
  auto comb_fin = [&](int qb) {
    const f32x4 a = ct0 + oa[0][qb], c = ct1 + oa[1][qb];
    *(LAS u32x4*)(uintptr_t)(lds0 + MISC + (w ^ 4) * 4096 + qb * 1024 + lane_id() * 16) = u32x4{cvt2(a[0], a[1]), cvt2(a[2], a[3]), cvt2(c[0], c[1]), cvt2(c[2], c[3])};
  };
  auto vstep = [&](int slot, int pn, auto cc) {
    constexpr int c = decltype(cc)::value;
    const unsigned vb = vaddr(slot);
    wbar<8>(); TS(c == 0 ? 2 : 1);
    if (hf == 1) issue_vw(pn);
    ring_step<16, (c == 0 ? NBV - 1 : NBV), 2>(carry, [&](int f) { return vrd(cc, vb, f); }, [&](int f, bf16x8 a) { vmm(cc, f, a); },
                          [&](int r, bf16x8 a) { wmm(7 - RDEF + r, a); }, [&]() { if (c == 0) comb_rd(0); },
                          [&](int f) { if (c == 0 && f == 2) { comb_fin(0); comb_rd(1); } if (c == 0 && f == 6) comb_fin(1); });
  };
  auto wstep = [&](int slot, auto pre, auto prev, auto mid, auto hook) {
    const int ln = lane_id(), rl = ((ln >> 4) << 2) | ((ln >> 2) & 3);
    unsigned wb = lds0 + slot * SS + rl * 128 + (((2 * nq + ((ln >> 1) & 1)) ^ (rl & 6)) << 4) + (ln & 1) * 8;
    OPAQUE(wb);
    pre();
    ring_step<7, NBW, NBW - RDEF>(carry, [&](int i) { return cat8(ldstr(wb + i * 4096), ldstr(wb + i * 4096 + 2048)); }, wmm, prev, mid, hook);
  };
#pragma unroll
  for (int r = 0; r < RDEF; ++r) { vmm(IC<3>{}, 16 - RDEF + r, carry[r]); carry[r] = (bf16x8)(0); }
  if (hf == 0) issue_vw(22);
  MFMA_DRAIN();
  comb_write(0); comb_write(1); comb_write(2); comb_write(3);
#pragma unroll 1
  for (int p = 20; p < 44; p += 8) {
    vstep(0, p + 3, IC<0>{}); if (hf == 0) issue_vw(p + 3);
    wstep(1, [&]() { wbar<8>(); TS(3); if (hf == 1) issue_vw(p + 4); oload(0); }, [&](int r, bf16x8 a) { vmm(IC<0>{}, 16 - RDEF + r, a); }, nop, nohook); if (hf == 0) issue_vw(p + 4);
    vstep(2, p + 5, IC<1>{}); if (hf == 0) issue_vw(p + 5);
    wstep(3, [&]() { wbar<8>(); TS(0); if (hf == 1) issue_vw(p + 6); oload(1); }, [&](int r, bf16x8 a) { vmm(IC<1>{}, 16 - RDEF + r, a); }, nop, nohook); if (hf == 0) issue_vw(p + 6);
    vstep(0, p + 7, IC<2>{}); if (hf == 0) issue_vw(p + 7);
    wstep(1, [&]() { wbar<8>(); TS(0); if (hf == 1) issue_vw(p + 8); oload(2); }, [&](int r, bf16x8 a) { vmm(IC<2>{}, 16 - RDEF + r, a); }, nop, nohook); if (hf == 0) issue_vw(p + 8);
    vstep(2, p + 9, IC<3>{}); oload(3); if (hf == 0) issue_vw(p + 9);
    wstep(3, [&]() { wbar<8>(); TS(0); if (hf == 1) issue_vw(p + 10); }, [&](int r, bf16x8 a) { vmm(IC<3>{}, 16 - RDEF + r, a); }, nop,
          [&](int f) { if (f >= 1) comb_write(f - 1); }); if (hf == 0) issue_vw(p + 10);
  }
  STAMP(3);
  CBAR();
  comb_rd(0);
#pragma unroll
  for (int r = 0; r < RDEF; ++r) wmm(7 - RDEF + r, carry[r]);
  comb_fin(0); comb_rd(1); issue_vw(47); comb_fin(1);
  unsigned long long* ctl = (unsigned long long*)(ws + (size_t)grp * 2048);
  const __amdgpu_buffer_rsrc_t wsr = __builtin_amdgcn_make_buffer_rsrc(ws, 0, 0x7fffffff, 0x00020000);
  const int lane = lane_id(), g = lane >> 4, li = lane & 15;
  unsigned long long ep;
  {
    const int rl = (g << 2) | ((lane >> 2) & 3);
    unsigned wb = lds0 + rl * 128 + (((2 * nq + ((lane >> 1) & 1)) ^ (rl & 6)) << 4) + (lane & 1) * 8, wb2 = wb + 2 * SS;
    PIN2(wb, wb2);
    wbar<0>();
    ep = __hip_atomic_load(ctl, __ATOMIC_RELAXED, __HIP_MEMORY_SCOPE_AGENT);
    bf16x8 of[4][2];
#pragma unroll
    for (int k = 0; k < 4; ++k) { oload(k); of[k][0] = o0; of[k][1] = o1; }
    auto st = [&](int i) {
      u32x4 f = {cvt2(pa[i][0][0], pa[i][0][1]), cvt2(pa[i][0][2], pa[i][0][3]), cvt2(pa[i][1][0], pa[i][1][1]), cvt2(pa[i][1][2], pa[i][1][3])};
      const unsigned off = (unsigned)WS_PART + (((unsigned)(grp * 56 + w * 7 + i) * 8 + kvh) << 10) + lane * 16;
      __builtin_amdgcn_raw_buffer_store_b128(f, wsr, off, 0, 16);
    };
    ring_step<28 + RDEF, 8, 6>(carry,
        [&](int f) { const int i = (f >> 2) % 7, k = f & 3; const unsigned a = ((k & 2) ? wb2 : wb) + (k & 1) * SS + i * 4096; return cat8(ldstr(a), ldstr(a + 2048)); },
        [&](int f, bf16x8 a) { mm2(pa[f >> 2][0], pa[f >> 2][1], a, of[f & 3][0], of[f & 3][1]); },
        [&](int, bf16x8) {}, nop,
        [&](int f) { if ((f & 3) == 2 && f >= 4) st((f >> 2) - 1); });
    MFMA_DRAIN();
    st(6);
  }
  const unsigned long long tag = ep * 0x9E3779B97F4A7C15ull + 0x632BE59BD9B4E019ull;
  STAMP(4);
  asm volatile("gqa_tail_label:");
  wbar<0>();
  STAMP(5);
  if (w == 0 && lane == 0) __hip_atomic_store(ctl + 16 * (kvh + 1), tag, __ATOMIC_RELAXED, __HIP_MEMORY_SCOPE_AGENT);
  if (w < 7) {
    for (unsigned spin = 0; spin < (1u << 22); ++spin) {
      unsigned long long f = tag;
      if (lane < 8) f = __hip_atomic_load(ctl + 16 * (lane + 1), __ATOMIC_RELAXED, __HIP_MEMORY_SCOPE_AGENT);
      if (__all(f == tag)) break;
      __builtin_amdgcn_s_sleep(2);
    }
    __builtin_amdgcn_fence(__ATOMIC_ACQUIRE, "wavefront");
    STAMP(6);
    const unsigned off = (unsigned)WS_PART + ((unsigned)(grp * 56 + kvh * 7 + w) << 13) + lane * 16;
    u32x4 v[8];
#pragma unroll
    for (int s = 0; s < 8; ++s) v[s] = __builtin_amdgcn_raw_buffer_load_b128(wsr, off + s * 1024, 0, 16);
    float acc[8];
#pragma unroll
    for (int e = 0; e < 8; ++e) acc[e] = 0.f;
#pragma unroll
    for (int s = 0; s < 8; ++s)
#pragma unroll
      for (int e = 0; e < 4; ++e) { acc[2 * e] += __uint_as_float(v[s][e] << 16); acc[2 * e + 1] += __uint_as_float(v[s][e] & 0xffff0000u); }
    const int q = 32 * (kvh >> 2) + li, n = 64 * w + 16 * (kvh & 3) + 4 * g;
    char* o = out + ((size_t)(b * SQ + q) * (2 * DM) + br * DM + n) * 2;
    *(u32x2*)o = u32x2{cvt2(acc[0], acc[1]), cvt2(acc[2], acc[3])};
    *(u32x2*)(o + 16 * 2 * DM * 2) = u32x2{cvt2(acc[4], acc[5]), cvt2(acc[6], acc[7])};
    STAMP(7);
    if (kvh == 0 && w == 0 && lane == 0) __hip_atomic_store(ctl, tag, __ATOMIC_RELAXED, __HIP_MEMORY_SCOPE_AGENT);
  }
}

extern "C" void kernel_launch(void* const* d_in, const int* in_sizes, int n_in, void* d_out, int out_size, void* d_ws, size_t ws_size,
                              hipStream_t stream) {
  (void)in_sizes; (void)n_in; (void)out_size; (void)ws_size;
  (void)hipFuncSetAttribute((const void*)gqa_fused_kernel, hipFuncAttributeMaxDynamicSharedMemorySize, LDS_BYTES);
  gqa_fused_kernel<<<256, 512, LDS_BYTES, stream>>>(
      (const char*)d_in[0], (const char*)d_in[1], (const char*)d_in[2], (const char*)d_in[3], (const char*)d_in[4],
      (const char*)d_in[5], (const char*)d_in[6], (const char*)d_in[7], (const char*)d_in[8], (const char*)d_in[9],
      (char*)d_out, (char*)d_ws);
}
```

```cpp
#include <hip/hip_runtime.h>
#include <hip/hip_bf16.h>
#include <stdint.h>
#include <type_traits>

typedef __attribute__((ext_vector_type(8))) short bf16x8;
typedef __attribute__((ext_vector_type(4))) short s16x4;
typedef __attribute__((ext_vector_type(4))) float f32x4;
typedef __attribute__((ext_vector_type(16))) float f32x16;
typedef __attribute__((ext_vector_type(2))) float f32x2;
typedef __attribute__((ext_vector_type(2))) __bf16 bf16x2_t;
typedef __attribute__((ext_vector_type(4))) unsigned u32x4;
typedef __attribute__((ext_vector_type(2))) unsigned u32x2;
#define LAS __attribute__((address_space(3)))
typedef LAS char* lds_ptr;

constexpr int SQ = 64, DM = 448, HKV = 8, SK = 1024, DH = 256;
constexpr int SS = 32768;
constexpr int MISC = 4 * SS;
constexpr int LDS_BYTES = 5 * SS;
constexpr int XIMG = 3 * SS;
constexpr int QST = 3 * SS;
constexpr float CEXP = 0.0625f * 1.4426950408889634f;
#define LMAX 1.0e18f
#define STAMP_ATT 0
#define NBK 5
#define NBV 6
#define NBW 4
constexpr int RDEF = 2;

constexpr size_t WS_PART = 65536;

#define MFMA16(a, b, c) __builtin_amdgcn_mfma_f32_16x16x32_bf16((a), (b), (c), 0, 0, 0)
#define MFMA32(a, b, c) __builtin_amdgcn_mfma_f32_32x32x16_bf16((a), (b), (c), 0, 0, 0)
#define CBAR() asm volatile("s_waitcnt lgkmcnt(0)\n\ts_barrier" ::: "memory")
#define SBAR() __builtin_amdgcn_sched_barrier(0)
#define OPAQUE(x) asm volatile("" : "+v"(x))
#define PIN2(x, y) asm volatile("" : "+v"(x), "+v"(y))
#define SOPAQUE(x) asm volatile("" : "+s"(x))
#define DI __device__ __forceinline__
template <int N> DI void wbar() { asm volatile("s_waitcnt vmcnt(%0) lgkmcnt(0)\n\ts_barrier" ::"i"(N) : "memory"); }
DI void bar() { asm volatile("s_barrier" ::: "memory"); }
template <int N> using IC = std::integral_constant<int, N>;

DI unsigned cvt2(float a, float b) { f32x2 v = {a, b}; return __builtin_bit_cast(unsigned, __builtin_convertvector(v, bf16x2_t)); }
DI bf16x8 ldsb128(unsigned a) { return *(const LAS bf16x8*)(uintptr_t)a; }
DI s16x4 ldstr(unsigned a) { return __builtin_amdgcn_ds_read_tr16_b64_v4i16((LAS s16x4*)(uintptr_t)a); }
DI bf16x8 cat8(s16x4 a, s16x4 b) { return __builtin_shufflevector(a, b, 0, 1, 2, 3, 4, 5, 6, 7); }
DI int lane_id() { int l; asm volatile("v_mbcnt_lo_u32_b32 %0, -1, 0\n\tv_mbcnt_hi_u32_b32 %0, -1, %0" : "=v"(l)); return l; }
DI float xsum(float v) {
  { auto r = __builtin_amdgcn_permlane16_swap(__float_as_uint(v), __float_as_uint(v), false, false); v = __uint_as_float(r[0]) + __uint_as_float(r[1]); }
  { auto r = __builtin_amdgcn_permlane32_swap(__float_as_uint(v), __float_as_uint(v), false, false); v = __uint_as_float(r[0]) + __uint_as_float(r[1]); }
  return v;
}
DI float xmax(float v) {
  { auto r = __builtin_amdgcn_permlane16_swap(__float_as_uint(v), __float_as_uint(v), false, false); v = fmaxf(__uint_as_float(r[0]), __uint_as_float(r[1])); }
  { auto r = __builtin_amdgcn_permlane32_swap(__float_as_uint(v), __float_as_uint(v), false, false); v = fmaxf(__uint_as_float(r[0]), __uint_as_float(r[1])); }
  return v;
}

DI void mm1(f32x4& c, bf16x8 a, bf16x8 b) { asm volatile("v_mfma_f32_16x16x32_bf16 %0, %1, %2, %0" : "+v"(c) : "v"(a), "v"(b)); }
DI void mm1z(f32x4& c, bf16x8 a, bf16x8 b) { asm volatile("v_mfma_f32_16x16x32_bf16 %0, %1, %2, 0" : "=&v"(c) : "v"(a), "v"(b)); }
DI void mm1c(f32x4& c, bf16x8 a, bf16x8 b, f32x4 i) { asm volatile("v_mfma_f32_16x16x32_bf16 %0, %1, %2, %3" : "=&v"(c) : "v"(a), "v"(b), "v"(i)); }
#define MFMA_DRAIN() do { asm volatile("s_nop 7\n\ts_nop 1" ::: "memory"); SBAR(); } while (0)

DI void dma4(unsigned lds, const void* sbase, unsigned v0, unsigned v1, unsigned v2, unsigned v3) {
  unsigned keep;
  asm volatile("s_mov_b32 %0, m0\n\ts_mov_b32 m0, %1\n\ts_nop 3\n\t"
               "global_load_lds_dwordx4 %2, %6\n\tglobal_load_lds_dwordx4 %3, %6 offset:1024\n\t"
               "global_load_lds_dwordx4 %4, %6 offset:2048\n\tglobal_load_lds_dwordx4 %5, %6 offset:3072\n\ts_mov_b32 m0, %0"
               : "=&s"(keep) : "s"(lds), "v"(v0), "v"(v1), "v"(v2), "v"(v3), "s"(sbase) : "memory");
}
DI void dma2(unsigned lds, const void* sbase, unsigned v0, unsigned v1) {
  unsigned keep;
  asm volatile("s_mov_b32 %0, m0\n\ts_mov_b32 m0, %1\n\ts_nop 3\n\t"
               "global_load_lds_dwordx4 %2, %4\n\tglobal_load_lds_dwordx4 %3, %4 offset:1024\n\ts_mov_b32 m0, %0"
               : "=&s"(keep) : "s"(lds), "v"(v0), "v"(v1), "s"(sbase) : "memory");
}
DI void dma4s(unsigned lds, const char* s, size_t gstride, unsigned v) {
  unsigned keep;
  asm volatile("s_mov_b32 %0, m0\n\ts_mov_b32 m0, %1\n\ts_nop 3\n\tglobal_load_lds_dwordx4 %2, %3 nt\n\t"
               "s_add_u32 m0, m0, 0x2000\n\ts_nop 0\n\tglobal_load_lds_dwordx4 %2, %4 nt\n\t"
               "s_add_u32 m0, m0, 0x2000\n\ts_nop 0\n\tglobal_load_lds_dwordx4 %2, %5 nt\n\t"
               "s_add_u32 m0, m0, 0x2000\n\ts_nop 0\n\tglobal_load_lds_dwordx4 %2, %6 nt\n\ts_mov_b32 m0, %0"
               : "=&s"(keep) : "s"(lds), "v"(v), "s"(s), "s"(s + gstride), "s"(s + 2 * gstride), "s"(s + 3 * gstride) : "memory", "scc");
}

DI void dma4s_keep(unsigned lds, const char* s, size_t gstride, unsigned v) {
  unsigned keep;
  asm volatile("s_mov_b32 %0, m0\n\ts_mov_b32 m0, %1\n\ts_nop 3\n\tglobal_load_lds_dwordx4 %2, %3\n\t"
               "s_add_u32 m0, m0, 0x400\n\ts_nop 0\n\tglobal_load_lds_dwordx4 %2, %4\n\t"
               "s_add_u32 m0, m0, 0x400\n\ts_nop 0\n\tglobal_load_lds_dwordx4 %2, %5\n\t"
               "s_add_u32 m0, m0, 0x400\n\ts_nop 0\n\tglobal_load_lds_dwordx4 %2, %6\n\ts_mov_b32 m0, %0"
               : "=&s"(keep) : "s"(lds), "v"(v), "s"(s), "s"(s + gstride), "s"(s + 2 * gstride), "s"(s + 3 * gstride) : "memory", "scc");
}

struct Frag { s16x4 lo, hi; };
template <int NF, int NB, int NI, class RD, class MM, class PREV, class MID, class HOOK>
DI void ring_step(Frag (&carry)[RDEF], RD rd, MM mm, PREV prev, MID mid, HOOK hook) {
  static_assert(NI + RDEF <= NB, "");
  Frag fb[NB];
#pragma unroll
  for (int f = 0; f < NI; ++f) { rd(f, 0, fb[f]); rd(f, 1, fb[f]); }
  SBAR();
#pragma unroll
  for (int r = 0; r < RDEF; ++r) {
    const bf16x8 a = cat8(carry[r].lo, carry[r].hi);
    prev(r, 0, a); SBAR(); if (r > 0) { rd(NI + r - 1, 0, fb[NI + r - 1]); SBAR(); }
    prev(r, 1, a); SBAR(); if (r > 0) { rd(NI + r - 1, 1, fb[NI + r - 1]); SBAR(); }
  }
  mid();
  SBAR();
#pragma unroll
  for (int f = NI + RDEF - 1; f < NB - 1; ++f) { rd(f, 0, fb[f]); rd(f, 1, fb[f]); }
  SBAR();
#pragma unroll
  for (int f = 0; f < NF - RDEF; ++f) {
    const bf16x8 a = cat8(fb[f % NB].lo, fb[f % NB].hi);
    const int g = f + NB - 1;
    mm(f, 0, a); SBAR(); if (g < NF) { rd(g, 0, fb[g % NB]); SBAR(); }
    mm(f, 1, a); SBAR(); if (g < NF) { rd(g, 1, fb[g % NB]); SBAR(); }
    hook(f); SBAR();
  }
#pragma unroll
  for (int r = 0; r < RDEF; ++r) carry[r] = fb[(NF - RDEF + r) % NB];
}

#define TAU 0
#define PACE(n) do { if (TAU && w == 7) { while ((unsigned)__builtin_amdgcn_s_memrealtime() - tk0 < (unsigned)((n) * TAU)) __builtin_amdgcn_s_sleep(1); } } while (0)
#define TS(k) do {} while (0)
#define STAMP(i) do {} while (0)

__global__ __launch_bounds__(512, 2) void gqa_fused_kernel(
    const char* __restrict__ xA, const char* __restrict__ kA, const char* __restrict__ vA, const char* __restrict__ wqA, const char* __restrict__ woA,
    const char* __restrict__ xB, const char* __restrict__ kB, const char* __restrict__ vB, const char* __restrict__ wqB, const char* __restrict__ woB,
    char* __restrict__ out, char* __restrict__ ws) {
  extern __shared__ __attribute__((aligned(16))) char smem[];
  const unsigned lds0 = (unsigned)(uintptr_t)(lds_ptr)smem;
  int w = __builtin_amdgcn_readfirstlane(threadIdx.x >> 6);
  const int id = blockIdx.x, br = (id >> 2) & 1, kvh = 2 * (id & 3) + ((id >> 3) & 1), b = id >> 4, grp = br * 16 + b;
  int rot7 = b % 7;
  int rg = w & 3, hf = w >> 2;
  STAMP(0);
  unsigned long long pc0; unsigned code_len;
  asm volatile("s_getpc_b64 %0\ngqa_head_label:\n\ts_mov_b32 %1, gqa_tail_label-gqa_head_label" : "=s"(pc0), "=s"(code_len));
  const char* kg = (br ? kB : kA) + (size_t)(b * HKV + kvh) * (SK * DH * 2);
  const char* vg = (br ? vB : vA) + (size_t)(b * HKV + kvh) * (SK * DH * 2);
  const char* wog = (br ? woB : woA) + (size_t)kvh * 512 * (DM * 2);

  auto k_eighth = [&](int t, int e) {
    const int ln = lane_id(), hi = ln >> 5;
    const char* kb = kg; SOPAQUE(kb);
    dma4s(lds0 + (t & 3) * SS + e * 1024, kb + (size_t)t * SS + e * 1024, 8192, hi * 512 + (((ln & 31) ^ (2 * e + hi)) << 4));
  };

  int vrot = (id >> 3) & 3;
  auto issue_vw = [&](int p) {
    const unsigned dst = lds0 + (p & 3) * SS + w * 1024;
    const int ln = lane_id();
    const bool isv = p < 20 || (p < 44 && !(p & 1));
    if (isv) {
      const int n = p < 20 ? p - 16 : 4 + ((p - 20) >> 1);
      const int r8 = ln >> 3;
      const char* vb = vg; SOPAQUE(vb);
      dma4s(dst, vb + (size_t)((n & 3) * 256 + 8 * w) * 512 + (((n >> 2) + vrot) & 3) * 128, 64 * 512, r8 * 512 + (((ln & 7) ^ (r8 & 6)) << 4));
    } else {
      const int k = p < 44 ? (p - 20) >> 1 : p - 32, r8 = ln >> 3;
      const char* wb = wog; SOPAQUE(wb);
      dma4s_keep(lds0 + (p & 3) * SS + w * 4096, wb + (size_t)(((k >> 1) & 1) * 256 + (((k >> 2) + vrot) & 3) * 64 + (k & 1) * 32) * 896 + (w < 7 ? w : 6) * 128,
                 8 * 896, r8 * 896 + (((ln & 7) ^ (r8 & 6)) << 4));
    }
  };
  auto issue = [&](int p) { if (p < 16) k_eighth(p, w); else issue_vw(p); };

  u32x4 pst[16][2];
  f32x4 oa[4][2];
  Frag carry[RDEF];
  float mref[2] = {-INFINITY, -INFINITY};
  unsigned tk0;
  float cpk;

  auto vaddr = [&](int slot) {
    const int ln = lane_id(), rl = ((ln >> 4) << 2) | ((ln >> 2) & 3);
    unsigned a = lds0 + slot * SS + hf * 4096 + rl * 128 + ((((ln >> 1) & 1) ^ (rl & 6) ^ (hf << 2)) << 4) + (ln & 1) * 8;
    OPAQUE(a);
    return a;
  };
  auto vu = [](int c, int f) { return c == 0 ? ((f >> 1) & 3) : (f >> 2); };
  auto vd = [](int c, int f) { return c == 0 ? ((f & 1) | ((f & 8) ? 0 : 2)) : (f & 3); };
  auto vrd = [&](auto cc, unsigned vb, int f, int h, Frag& d) {
    constexpr int c = decltype(cc)::value;
    const unsigned a = (vb ^ (unsigned)(vd(c, f) << 5)) + vu(c, f) * 8192;
    if (h == 0) d.lo = ldstr(a); else d.hi = ldstr(a + 2048);
  };
  auto vmm = [&](auto cc, int f, int qb, bf16x8 a) {
    constexpr int c = decltype(cc)::value;
    const int u = vu(c, f), db = vd(c, f);
    const bf16x8 b = __builtin_bit_cast(bf16x8, pst[4 * c + u][qb]);
    if (c == 0 && u == 0) mm1z(oa[db][qb], a, b); else mm1(oa[db][qb], a, b);
  };
  auto nohook = [](int) {};
  auto nop = []() {};

  for (int attempt = 0;; ++attempt) {
  SOPAQUE(w); SOPAQUE(rot7); SOPAQUE(vrot); rg = w & 3; hf = w >> 2;
  if (__builtin_expect(w >= 4, 1)) {
    const int lw = w - 4, lane = lane_id();
    const char* xg = (br ? xB : xA) + (size_t)b * (SQ * DM * 2); SOPAQUE(xg);
    const char* wqg = (br ? wqB : wqA) + (size_t)kvh * 1024; SOPAQUE(wqg);
    unsigned wofs[4];
#pragma unroll
    for (int j = 0; j < 4; ++j) wofs[j] = j * (8192 - 1024) + ((lane ^ (j << 2)) << 4);
    int wsl = 1;
    auto wtile = [&](int i) {
      int G = (i >> 1) + rot7; G = G >= 7 ? G - 7 : G;
      const char* src = wqg + (size_t)(G * 64 + (i & 1) * 32 + 8 * lw) * 8192;
      const unsigned dst = lds0 + wsl * SS + lw * 8192;
      dma4(dst, src, wofs[0], wofs[1], wofs[2], wofs[3]);
      dma4(dst + 4096, src + 4 * 8192, wofs[0], wofs[1], wofs[2], wofs[3]);
      wsl = wsl == 2 ? 0 : wsl + 1;
    };
    const int xr = 16 * lw + (lane >> 3), xc = lane & 7;
    const unsigned x0 = xr * 896 + ((xc ^ ((xr >> 1) & 7)) << 4), x1 = (xr + 8) * 896 + ((xc ^ (((xr + 8) >> 1) & 7)) << 4) - 1024;
    auto xtile = [&](int Gi) { int G = Gi + rot7; G = G >= 7 ? G - 7 : G; dma2(lds0 + XIMG + Gi * 8192 + lw * 2048, xg + G * 128, x0, x1); };
    xtile(0); wtile(0); wtile(1);
    for (int i = 0; i < 14; ++i) {
      if (i == 1) wbar<20>(); else wbar<8>();
      if (i == 0) for (int Gi = 1; Gi < 7; ++Gi) xtile(Gi);
      if (i + 2 < 14) wtile(i + 2); else { k_eighth(i - 12, 2 * lw); k_eighth(i - 12, 2 * lw + 1); }
    }
  } else {
    const int lane = lane_id(), g = lane >> 4, qq = (lane >> 2) & 3, pp = lane & 3;
    const int mh = w & 1, hh = w >> 1, h = lane >> 5, m = lane & 31;
    f32x16 qa[8];
#pragma unroll
    for (int a = 0; a < 8; ++a) qa[a] = (f32x16)(0.f);
    const unsigned wbase = lds0 + (8 * h + qq) * 1024 + hh * 512 + (((2 * (g & 1) + (pp >> 1)) ^ (qq << 2)) << 4) + (pp & 1) * 8;
    const unsigned xbase = lds0 + XIMG + (32 * mh + m) * 128 + ((h ^ ((m >> 1) & 7)) << 4);
    bf16x8 wfA[8], wfB[8], xa, xb, xo, xc = (bf16x8)(0), xd = (bf16x8)(0);
#pragma unroll
    for (int a = 0; a < 8; ++a) wfB[a] = (bf16x8)(0);
    xo = (bf16x8)(0);
    int sl = SS;
    unsigned tch = 0, tch2 = 0;
    for (int i = 0; i < 14; ++i) {
      CBAR();
      if (i == 0 && code_len < 65536u) {
        unsigned o = (w * 64 + lane) * 128; o = o < code_len ? o : 0;
        asm volatile("global_load_dword %0, %1, %2" : "=v"(tch) : "v"(o), "s"(pc0) : "memory");
        o = 32768 + (w * 64 + lane) * 128; o = o < code_len ? o : 0;
        asm volatile("global_load_dword %0, %1, %2" : "=v"(tch2) : "v"(o), "s"(pc0) : "memory");
      }
      unsigned p = wbase + sl; OPAQUE(p);
      sl = sl == 2 * SS ? 0 : sl + SS;
      const unsigned xp = (xbase + (i >> 1) * 8192) ^ (unsigned)((i & 1) << 6);
      if (i < 13) { xa = ldsb128(xp); xb = ldsb128(xp ^ 32); } else { xa = xc; xb = xd; }
      if (i == 12) { xc = ldsb128(xp ^ 64); xd = ldsb128(xp ^ 96); }
      SBAR();
#pragma unroll
      for (int a = 0; a < 8; ++a) {
        const unsigned q = (p ^ (unsigned)((a & 3) << 6)) + (a >> 2) * 256; wfA[a] = cat8(ldstr(q), ldstr(q + 4096));
        qa[a] = MFMA32(wfB[a], xo, qa[a]);
        SBAR();
      }
#pragma unroll
      for (int a = 0; a < 8; ++a) {
        if (a < 4) {
#pragma unroll
          for (int u = 0; u < 2; ++u) { const int a2 = 2 * a + u; const unsigned q = (p ^ (unsigned)((a2 & 3) << 6)) + (a2 >> 2) * 256; wfB[a2] = cat8(ldstr(q + 16384), ldstr(q + 16384 + 4096)); }
        }
        qa[a] = MFMA32(wfA[a], xa, qa[a]);
        SBAR();
      }
      xo = xb;
    }
    asm volatile("s_waitcnt vmcnt(0)" : "+v"(tch), "+v"(tch2) :: "memory");
    const unsigned qw = lds0 + QST + (hh * 2 + mh) * 16384 + (m >> 4) * 8192 + (m & 15) * 16 + h * 8;
#pragma unroll
    for (int nb = 0; nb < 8; ++nb) {
      qa[nb] = MFMA32(wfB[nb], xo, qa[nb]);
#pragma unroll
      for (int a = 0; a < 4; ++a)
        *(LAS u32x2*)(uintptr_t)(qw + nb * 1024 + a * 256) =
            u32x2{cvt2(qa[nb][4 * a] * CEXP, qa[nb][4 * a + 1] * CEXP), cvt2(qa[nb][4 * a + 2] * CEXP, qa[nb][4 * a + 3] * CEXP)};
    }
  }
  CBAR();
  bf16x8 qf[2][8];
  {
    const unsigned qr = lds0 + QST + rg * 16384 + lane_id() * 16;
#pragma unroll
    for (int qb = 0; qb < 2; ++qb)
#pragma unroll
      for (int s = 0; s < 8; ++s) qf[qb][s] = ldsb128(qr + (qb * 8 + s) * 1024);
  }
  if (attempt == STAMP_ATT) STAMP(1);

    tk0 = (unsigned)__builtin_amdgcn_s_memrealtime();
    f32x4 sa[2][2];
    f32x4 nm[2];
    float tmax[2] = {0.f, 0.f}, lsum[2] = {0.f, 0.f};
    auto kmm = [&](auto tt, int f, int qb, bf16x8 a) {
      constexpr int t = decltype(tt)::value;
      if (f >= 2) mm1(sa[f & 1][qb], a, qf[qb][f >> 1]);
      else if (t == 0) mm1z(sa[f][qb], a, qf[qb][0]);
      else mm1c(sa[f][qb], a, qf[qb][0], nm[qb]);
    };
    auto smax = [&](int qb) {
      return fmaxf(fmaxf(fmaxf(sa[0][qb][0], sa[0][qb][1]), fmaxf(sa[0][qb][2], sa[0][qb][3])), fmaxf(fmaxf(sa[1][qb][0], sa[1][qb][1]), fmaxf(sa[1][qb][2], sa[1][qb][3])));
    };
    auto soft = [&](auto tt) {
      constexpr int t = decltype(tt)::value;
      MFMA_DRAIN();
#pragma unroll
      for (int qb = 0; qb < 2; ++qb) {
        if (t == 0) {
          mref[qb] = fmaxf(mref[qb], xmax(smax(qb)));
          nm[qb] = (f32x4)(-mref[qb]); OPAQUE(nm[qb]);
          sa[0][qb] += nm[qb]; sa[1][qb] += nm[qb];
        } else if (attempt == 1) tmax[qb] = fmaxf(tmax[qb], smax(qb));
        float e[8];
#pragma unroll
        for (int r = 0; r < 8; ++r) e[r] = __builtin_amdgcn_exp2f(sa[r >> 2][qb][r & 3]);
        lsum[qb] += ((e[0] + e[1]) + (e[2] + e[3])) + ((e[4] + e[5]) + (e[6] + e[7])); OPAQUE(lsum[qb]);
        pst[t][qb] = u32x4{cvt2(e[0], e[1]), cvt2(e[2], e[3]), cvt2(e[4], e[5]), cvt2(e[6], e[7])};
        OPAQUE(pst[t][qb]);
      }
    };
    auto kstep = [&](auto tt) {
      constexpr int t = decltype(tt)::value;
      const int ln = lane_id();
      unsigned kb0 = lds0 + (t & 3) * SS + hf * 16384 + (ln & 15) * 512 + (((ln >> 4) ^ (ln & 15)) << 4); OPAQUE(kb0);
      if (t > 1) PACE(t - 1);
      if (t == 0) wbar<8>(); else wbar<4>();
      if (hf == 1) issue(t + 2);
      ring_step<16, NBK, 2>(carry,
          [&](int f, int h, Frag& d) {
            if (h == 0) { const bf16x8 v = ldsb128((kb0 ^ (unsigned)(((f >> 1) & 3) << 6)) + (f >> 3) * 256 + (f & 1) * 8192);
                          d.lo = __builtin_shufflevector(v, v, 0, 1, 2, 3); d.hi = __builtin_shufflevector(v, v, 4, 5, 6, 7); } },
          [&](int f, int qb, bf16x8 a) { kmm(IC<t>{}, f, qb, a); },
          [&](int r, int qb, bf16x8 a) { if constexpr (t > 0) kmm(IC<t - 1>{}, 16 - RDEF + r, qb, a); },
          [&]() { if constexpr (t > 0) soft(IC<t - 1>{}); }, nohook);
      if (hf == 0) issue(t + 2);
    };
    kstep(IC<0>{}); kstep(IC<1>{}); kstep(IC<2>{}); kstep(IC<3>{}); kstep(IC<4>{}); kstep(IC<5>{}); kstep(IC<6>{}); kstep(IC<7>{});
    kstep(IC<8>{}); kstep(IC<9>{}); kstep(IC<10>{}); kstep(IC<11>{}); kstep(IC<12>{}); kstep(IC<13>{}); kstep(IC<14>{}); kstep(IC<15>{});
    if (attempt == STAMP_ATT) STAMP(2);
    {
      const unsigned vb = vaddr(0); PACE(15); wbar<4>(); if (hf == 1) { issue_vw(18); issue_vw(19); }
      ring_step<16, NBV, 2>(carry, [&](int f, int h, Frag& d) { vrd(IC<0>{}, vb, f, h, d); }, [&](int f, int qb, bf16x8 a) { vmm(IC<0>{}, f, qb, a); },
                            [&](int r, int qb, bf16x8 a) { kmm(IC<15>{}, 16 - RDEF + r, qb, a); },
                            [&]() {
                              soft(IC<15>{});
                              const int ln = lane_id();
                              lsum[0] = xsum(lsum[0]); lsum[1] = xsum(lsum[1]);
                              const bool bad = !(lsum[0] <= LMAX) || !(lsum[1] <= LMAX);
                              const float anybad = __any(bad) ? 1.f : 0.f;
                              if (ln < 16) *(LAS f32x4*)(uintptr_t)(lds0 + MISC + w * 256 + ln * 16) = f32x4{mref[0], lsum[0], mref[1], lsum[1]};
                              if (ln == 0) *(LAS float*)(uintptr_t)(lds0 + MISC + 2048 + w * 4) = anybad;
                            }, nohook);
      if (hf == 0) { issue_vw(18); issue_vw(19); }
    }
    float fl;
    {
      const unsigned vb = vaddr(1); PACE(16); wbar<8>(); if (hf == 1) issue_vw(20);
      ring_step<16, NBV, 2>(carry, [&](int f, int h, Frag& d) { vrd(IC<1>{}, vb, f, h, d); }, [&](int f, int qb, bf16x8 a) { vmm(IC<1>{}, f, qb, a); },
                         [&](int r, int qb, bf16x8 a) { vmm(IC<0>{}, 16 - RDEF + r, qb, a); },
                         [&]() {
                           const int ln = lane_id();
                           const f32x4 o = *(const LAS f32x4*)(uintptr_t)(lds0 + MISC + (w ^ 4) * 256 + (ln & 15) * 16);
                           const f32x4 f0 = *(const LAS f32x4*)(uintptr_t)(lds0 + MISC + 2048), f1 = *(const LAS f32x4*)(uintptr_t)(lds0 + MISC + 2048 + 16);
                           fl = (f0[0] + f0[1]) + (f0[2] + f0[3]) + (f1[0] + f1[1]) + (f1[2] + f1[3]); OPAQUE(fl);
                           const bool odd = (ln >> 4) & 1;
                           float l0 = lsum[0], l1 = lsum[1], m0 = mref[0], m1 = mref[1], o0_ = o[0], o1_ = o[1], o2_ = o[2], o3_ = o[3];
                           OPAQUE(l0); OPAQUE(l1); OPAQUE(m0); OPAQUE(m1); OPAQUE(o0_); OPAQUE(o1_); OPAQUE(o2_); OPAQUE(o3_);
                           const float ms = odd ? m1 : m0, ls = odd ? l1 : l0, mo = odd ? o2_ : o0_, lo = odd ? o3_ : o1_;
                           const float m = fmaxf(ms, mo), ea = __builtin_amdgcn_exp2f(ms - m), eb = __builtin_amdgcn_exp2f(mo - m);
                           cpk = ea / (ls * ea + lo * eb); OPAQUE(cpk);
                         }, nohook);
      if (hf == 0) issue_vw(20);
    }
    if (attempt == 2 || (attempt == 0 && __builtin_amdgcn_readfirstlane(__float_as_uint(fl)) == 0u)) break;
    mref[0] += xmax(tmax[0]); mref[1] += xmax(tmax[1]);
    wbar<0>();
  }
  {
    const unsigned vb = vaddr(2); PACE(17); wbar<8>(); if (hf == 1) issue_vw(21);
    ring_step<16, NBV, 2>(carry, [&](int f, int h, Frag& d) { vrd(IC<2>{}, vb, f, h, d); }, [&](int f, int qb, bf16x8 a) { vmm(IC<2>{}, f, qb, a); },
                          [&](int r, int qb, bf16x8 a) { vmm(IC<1>{}, 16 - RDEF + r, qb, a); }, nop, nohook);
    if (hf == 0) issue_vw(21);
  }
  {
    const unsigned vb = vaddr(3); PACE(18); wbar<8>(); if (hf == 1) issue_vw(22);
    ring_step<16, NBV, 2>(carry, [&](int f, int h, Frag& d) { vrd(IC<3>{}, vb, f, h, d); }, [&](int f, int qb, bf16x8 a) { vmm(IC<3>{}, f, qb, a); },
                          [&](int r, int qb, bf16x8 a) { vmm(IC<2>{}, 16 - RDEF + r, qb, a); }, nop, nohook);
  }

  const int qh = w >> 2, nq = w & 3;
  f32x4 pa[7][2];
#pragma unroll
  for (int i = 0; i < 7; ++i) { pa[i][0] = (f32x4)(0.f); pa[i][1] = (f32x4)(0.f); }
  bf16x8 o0 = (bf16x8)(0), o1 = (bf16x8)(0);
  auto oload = [&](int idx) {
    const unsigned oad = lds0 + MISC + ((((idx >> 1) * 2 + qh) + 4 * ((idx & 1) ^ 1)) * 4096) + lane_id() * 16;
    o0 = ldsb128(oad); o1 = ldsb128(oad + 1024);
  };
  auto wmm = [&](int i, int qb, bf16x8 a) { mm1(pa[i][qb], a, qb ? o1 : o0); };
  float c0, c1;
  auto comb_write = [&](int k) {
    if (k == 0) {
      float cc = cpk; OPAQUE(cc);
      const auto cr = __builtin_amdgcn_permlane16_swap(__float_as_uint(cc), __float_as_uint(cc), false, false);
      c0 = __uint_as_float(cr[0]); c1 = __uint_as_float(cr[1]);
    }
    if (k < 2) {
      const unsigned ad = lds0 + MISC + w * 4096 + k * 2048 + lane_id() * 16;
      *(LAS f32x4*)(uintptr_t)(ad) = oa[2 + k][0] * c0; *(LAS f32x4*)(uintptr_t)(ad + 1024) = oa[2 + k][1] * c1;
    } else if (k < 4) { oa[k - 2][0] *= c0; oa[k - 2][1] *= c1; PIN2(oa[k - 2][0], oa[k - 2][1]); }
  };
  f32x4 ct0, ct1; u32x4 cf0;
  auto comb_rd = [&](int qb) {
    const unsigned pad = lds0 + MISC + (w ^ 4) * 4096 + qb * 1024 + lane_id() * 16;
    ct0 = *(const LAS f32x4*)(uintptr_t)(pad); ct1 = *(const LAS f32x4*)(uintptr_t)(pad + 2048);
  };
  auto comb_fin = [&](int qb) {
    const f32x4 a = ct0 + oa[0][qb], c = ct1 + oa[1][qb];
    *(LAS u32x4*)(uintptr_t)(lds0 + MISC + (w ^ 4) * 4096 + qb * 1024 + lane_id() * 16) = u32x4{cvt2(a[0], a[1]), cvt2(a[2], a[3]), cvt2(c[0], c[1]), cvt2(c[2], c[3])};
  };
  auto vstep = [&](int slot, int pn, auto cc) {
    constexpr int c = decltype(cc)::value;
    const unsigned vb = vaddr(slot);
    wbar<8>(); TS(c == 0 ? 2 : 1);
    if (hf == 1) issue_vw(pn);
    ring_step<16, (c == 0 ? NBV - 1 : NBV), 2>(carry, [&](int f, int h, Frag& d) { vrd(cc, vb, f, h, d); }, [&](int f, int qb, bf16x8 a) { vmm(cc, f, qb, a); },
                          [&](int r, int qb, bf16x8 a) { wmm(7 - RDEF + r, qb, a); }, [&]() { if (c == 0) comb_rd(0); },
                          [&](int f) { if (c == 0 && f == 2) { comb_fin(0); comb_rd(1); } if (c == 0 && f == 6) comb_fin(1); });
  };
  auto wstep = [&](int slot, auto pre, auto prev, auto mid, auto hook) {
    const int ln = lane_id(), rl = ((ln >> 4) << 2) | ((ln >> 2) & 3);
    unsigned wb = lds0 + slot * SS + rl * 128 + (((2 * nq + ((ln >> 1) & 1)) ^ (rl & 6)) << 4) + (ln & 1) * 8;
    OPAQUE(wb);
    pre();
    ring_step<7, NBW, NBW - RDEF>(carry, [&](int i, int h, Frag& d) { if (h == 0) d.lo = ldstr(wb + i * 4096); else d.hi = ldstr(wb + i * 4096 + 2048); }, wmm, prev, mid, hook);
  };
#pragma unroll
  for (int r = 0; r < RDEF; ++r) { const bf16x8 a = cat8(carry[r].lo, carry[r].hi); vmm(IC<3>{}, 16 - RDEF + r, 0, a); vmm(IC<3>{}, 16 - RDEF + r, 1, a); carry[r].lo = (s16x4)(0); carry[r].hi = (s16x4)(0); }
  if (hf == 0) issue_vw(22);
  MFMA_DRAIN();
  comb_write(0); comb_write(1); comb_write(2); comb_write(3);
#pragma unroll 1
  for (int p = 20; p < 44; p += 8) {
    vstep(0, p + 3, IC<0>{}); if (hf == 0) issue_vw(p + 3);
    wstep(1, [&]() { wbar<8>(); TS(3); if (hf == 1) issue_vw(p + 4); oload(0); }, [&](int r, int qb, bf16x8 a) { vmm(IC<0>{}, 16 - RDEF + r, qb, a); }, nop, nohook); if (hf == 0) issue_vw(p + 4);
    vstep(2, p + 5, IC<1>{}); if (hf == 0) issue_vw(p + 5);
    wstep(3, [&]() { wbar<8>(); TS(0); if (hf == 1) issue_vw(p + 6); oload(1); }, [&](int r, int qb, bf16x8 a) { vmm(IC<1>{}, 16 - RDEF + r, qb, a); }, nop, nohook); if (hf == 0) issue_vw(p + 6);
    vstep(0, p + 7, IC<2>{}); if (hf == 0) issue_vw(p + 7);
    wstep(1, [&]() { wbar<8>(); TS(0); if (hf == 1) issue_vw(p + 8); oload(2); }, [&](int r, int qb, bf16x8 a) { vmm(IC<2>{}, 16 - RDEF + r, qb, a); }, nop, nohook); if (hf == 0) issue_vw(p + 8);
    vstep(2, p + 9, IC<3>{}); oload(3); if (hf == 0) issue_vw(p + 9);
    wstep(3, [&]() { wbar<8>(); TS(0); if (hf == 1) issue_vw(p + 10); }, [&](int r, int qb, bf16x8 a) { vmm(IC<3>{}, 16 - RDEF + r, qb, a); }, nop,
          [&](int f) { if (f >= 1) comb_write(f - 1); }); if (hf == 0) issue_vw(p + 10);
  }
  STAMP(3);
  CBAR();
  comb_rd(0);
#pragma unroll
  for (int r = 0; r < RDEF; ++r) { const bf16x8 a = cat8(carry[r].lo, carry[r].hi); wmm(7 - RDEF + r, 0, a); wmm(7 - RDEF + r, 1, a); }
  comb_fin(0); comb_rd(1); issue_vw(47); comb_fin(1);
  unsigned long long* ctl = (unsigned long long*)(ws + (size_t)grp * 2048);
  const __amdgpu_buffer_rsrc_t wsr = __builtin_amdgcn_make_buffer_rsrc(ws, 0, 0x7fffffff, 0x00020000);
  const int lane = lane_id(), g = lane >> 4, li = lane & 15;
  unsigned long long ep;
  {
    const int rl = (g << 2) | ((lane >> 2) & 3);
    unsigned wb = lds0 + rl * 128 + (((2 * nq + ((lane >> 1) & 1)) ^ (rl & 6)) << 4) + (lane & 1) * 8, wb2 = wb + 2 * SS;
    PIN2(wb, wb2);
    wbar<0>();
    ep = __hip_atomic_load(ctl, __ATOMIC_RELAXED, __HIP_MEMORY_SCOPE_AGENT);
    bf16x8 of[4][2];
#pragma unroll
    for (int k = 0; k < 4; ++k) { oload(k); of[k][0] = o0; of[k][1] = o1; }
    auto st = [&](int i) {
      u32x4 f = {cvt2(pa[i][0][0], pa[i][0][1]), cvt2(pa[i][0][2], pa[i][0][3]), cvt2(pa[i][1][0], pa[i][1][1]), cvt2(pa[i][1][2], pa[i][1][3])};
      const unsigned off = (unsigned)WS_PART + (((unsigned)(grp * 56 + w * 7 + i) * 8 + kvh) << 10) + lane * 16;
      __builtin_amdgcn_raw_buffer_store_b128(f, wsr, off, 0, 16);
    };
    ring_step<28 + RDEF, 8, 6>(carry,
        [&](int f, int h, Frag& d) { const int i = (f >> 2) % 7, k = f & 3; const unsigned a = ((k & 2) ? wb2 : wb) + (k & 1) * SS + i * 4096; if (h == 0) d.lo = ldstr(a); else d.hi = ldstr(a + 2048); },
        [&](int f, int qb, bf16x8 a) { mm1(pa[f >> 2][qb], a, of[f & 3][qb]); },
        [&](int, int, bf16x8) {}, nop,
        [&](int f) { if ((f & 3) == 2 && f >= 4) st((f >> 2) - 1); });
    MFMA_DRAIN();
    st(6);
  }
  const unsigned long long tag = ep * 0x9E3779B97F4A7C15ull + 0x632BE59BD9B4E019ull;
  STAMP(4);
  asm volatile("gqa_tail_label:");
  wbar<0>();
  STAMP(5);
  if (w == 0 && lane == 0) __hip_atomic_store(ctl + 16 * (kvh + 1), tag, __ATOMIC_RELAXED, __HIP_MEMORY_SCOPE_AGENT);
  if (w < 7) {
    for (unsigned spin = 0; spin < (1u << 22); ++spin) {
      unsigned long long f = tag;
      if (lane < 8) f = __hip_atomic_load(ctl + 16 * (lane + 1), __ATOMIC_RELAXED, __HIP_MEMORY_SCOPE_AGENT);
      if (__all(f == tag)) break;
      __builtin_amdgcn_s_sleep(2);
    }
    __builtin_amdgcn_fence(__ATOMIC_ACQUIRE, "wavefront");
    STAMP(6);
    const unsigned off = (unsigned)WS_PART + ((unsigned)(grp * 56 + kvh * 7 + w) << 13) + lane * 16;
    u32x4 v[8];
#pragma unroll
    for (int s = 0; s < 8; ++s) v[s] = __builtin_amdgcn_raw_buffer_load_b128(wsr, off + s * 1024, 0, 16);
    float acc[8];
#pragma unroll
    for (int e = 0; e < 8; ++e) acc[e] = 0.f;
#pragma unroll
    for (int s = 0; s < 8; ++s)
#pragma unroll
      for (int e = 0; e < 4; ++e) { acc[2 * e] += __uint_as_float(v[s][e] << 16); acc[2 * e + 1] += __uint_as_float(v[s][e] & 0xffff0000u); }
    const int q = 32 * (kvh >> 2) + li, n = 64 * w + 16 * (kvh & 3) + 4 * g;
    char* o = out + ((size_t)(b * SQ + q) * (2 * DM) + br * DM + n) * 2;
    *(u32x2*)o = u32x2{cvt2(acc[0], acc[1]), cvt2(acc[2], acc[3])};
    *(u32x2*)(o + 16 * 2 * DM * 2) = u32x2{cvt2(acc[4], acc[5]), cvt2(acc[6], acc[7])};
    STAMP(7);
    if (kvh == 0 && w == 0 && lane == 0) __hip_atomic_store(ctl, tag, __ATOMIC_RELAXED, __HIP_MEMORY_SCOPE_AGENT);
  }
}

extern "C" void kernel_launch(void* const* d_in, const int* in_sizes, int n_in, void* d_out, int out_size, void* d_ws, size_t ws_size,
                              hipStream_t stream) {
  (void)in_sizes; (void)n_in; (void)out_size; (void)ws_size;
  (void)hipFuncSetAttribute((const void*)gqa_fused_kernel, hipFuncAttributeMaxDynamicSharedMemorySize, LDS_BYTES);
  gqa_fused_kernel<<<256, 512, LDS_BYTES, stream>>>(
      (const char*)d_in[0], (const char*)d_in[1], (const char*)d_in[2], (const char*)d_in[3], (const char*)d_in[4],
      (const char*)d_in[5], (const char*)d_in[6], (const char*)d_in[7], (const char*)d_in[8], (const char*)d_in[9],
      (char*)d_out, (char*)d_ws);
}
```

```cpp
#include <hip/hip_runtime.h>
#include <hip/hip_bf16.h>
#include <stdint.h>
#include <type_traits>

typedef __attribute__((ext_vector_type(8))) short bf16x8;
typedef __attribute__((ext_vector_type(4))) short s16x4;
typedef __attribute__((ext_vector_type(4))) float f32x4;
typedef __attribute__((ext_vector_type(16))) float f32x16;
typedef __attribute__((ext_vector_type(2))) float f32x2;
typedef __attribute__((ext_vector_type(2))) __bf16 bf16x2_t;
typedef __attribute__((ext_vector_type(4))) unsigned u32x4;
typedef __attribute__((ext_vector_type(2))) unsigned u32x2;
#define LAS __attribute__((address_space(3)))
typedef LAS char* lds_ptr;

constexpr int SQ = 64, DM = 448, HKV = 8, SK = 1024, DH = 256;
constexpr int SS = 32768;
constexpr int MISC = 4 * SS;
constexpr int LDS_BYTES = 5 * SS;
constexpr int XIMG = 3 * SS;
constexpr int QST = 3 * SS;
constexpr float CEXP = 0.0625f * 1.4426950408889634f;
#define LMAX 1.0e12f
#define NBK 5
#define NBV 6
#define NBW 4
constexpr int RDEF = 2;

constexpr size_t WS_PART = 65536;

#define MFMA16(a, b, c) __builtin_amdgcn_mfma_f32_16x16x32_bf16((a), (b), (c), 0, 0, 0)
#define MFMA32(a, b, c) __builtin_amdgcn_mfma_f32_32x32x16_bf16((a), (b), (c), 0, 0, 0)
#define CBAR() asm volatile("s_waitcnt lgkmcnt(0)\n\ts_barrier" ::: "memory")
#define SBAR() __builtin_amdgcn_sched_barrier(0)
#define OPAQUE(x) asm volatile("" : "+v"(x))
#define PIN2(x, y) asm volatile("" : "+v"(x), "+v"(y))
#define SOPAQUE(x) asm volatile("" : "+s"(x))
#define DI __device__ __forceinline__
template <int N> DI void wbar() { asm volatile("s_waitcnt vmcnt(%0) lgkmcnt(0)\n\ts_barrier" ::"i"(N) : "memory"); }
DI void bar() { asm volatile("s_barrier" ::: "memory"); }
template <int N> using IC = std::integral_constant<int, N>;

DI unsigned cvt2(float a, float b) { f32x2 v = {a, b}; return __builtin_bit_cast(unsigned, __builtin_convertvector(v, bf16x2_t)); }
DI bf16x8 ldsb128(unsigned a) { return *(const LAS bf16x8*)(uintptr_t)a; }
DI s16x4 ldstr(unsigned a) { return __builtin_amdgcn_ds_read_tr16_b64_v4i16((LAS s16x4*)(uintptr_t)a); }
DI bf16x8 cat8(s16x4 a, s16x4 b) { return __builtin_shufflevector(a, b, 0, 1, 2, 3, 4, 5, 6, 7); }
DI int lane_id() { int l; asm volatile("v_mbcnt_lo_u32_b32 %0, -1, 0\n\tv_mbcnt_hi_u32_b32 %0, -1, %0" : "=v"(l)); return l; }
DI float xsum(float v) {
  { auto r = __builtin_amdgcn_permlane16_swap(__float_as_uint(v), __float_as_uint(v), false, false); v = __uint_as_float(r[0]) + __uint_as_float(r[1]); }
  { auto r = __builtin_amdgcn_permlane32_swap(__float_as_uint(v), __float_as_uint(v), false, false); v = __uint_as_float(r[0]) + __uint_as_float(r[1]); }
  return v;
}
DI float xmax(float v) {
  { auto r = __builtin_amdgcn_permlane16_swap(__float_as_uint(v), __float_as_uint(v), false, false); v = fmaxf(__uint_as_float(r[0]), __uint_as_float(r[1])); }
  { auto r = __builtin_amdgcn_permlane32_swap(__float_as_uint(v), __float_as_uint(v), false, false); v = fmaxf(__uint_as_float(r[0]), __uint_as_float(r[1])); }
  return v;
}

DI void mm1(f32x4& c, bf16x8 a, bf16x8 b) { asm volatile("v_mfma_f32_16x16x32_bf16 %0, %1, %2, %0" : "+v"(c) : "v"(a), "v"(b)); }
DI void mm1z(f32x4& c, bf16x8 a, bf16x8 b) { asm volatile("v_mfma_f32_16x16x32_bf16 %0, %1, %2, 0" : "=&v"(c) : "v"(a), "v"(b)); }
DI void mm1c(f32x4& c, bf16x8 a, bf16x8 b, f32x4 i) { asm volatile("v_mfma_f32_16x16x32_bf16 %0, %1, %2, %3" : "=&v"(c) : "v"(a), "v"(b), "v"(i)); }
#define MFMA_DRAIN() do { asm volatile("s_nop 7\n\ts_nop 1" ::: "memory"); SBAR(); } while (0)

DI void dma4(unsigned lds, const void* sbase, unsigned v0, unsigned v1, unsigned v2, unsigned v3) {
  unsigned keep;
  asm volatile("s_mov_b32 %0, m0\n\ts_mov_b32 m0, %1\n\ts_nop 3\n\t"
               "global_load_lds_dwordx4 %2, %6\n\tglobal_load_lds_dwordx4 %3, %6 offset:1024\n\t"
               "global_load_lds_dwordx4 %4, %6 offset:2048\n\tglobal_load_lds_dwordx4 %5, %6 offset:3072\n\ts_mov_b32 m0, %0"
               : "=&s"(keep) : "s"(lds), "v"(v0), "v"(v1), "v"(v2), "v"(v3), "s"(sbase) : "memory");
}
DI void dma2(unsigned lds, const void* sbase, unsigned v0, unsigned v1) {
  unsigned keep;
  asm volatile("s_mov_b32 %0, m0\n\ts_mov_b32 m0, %1\n\ts_nop 3\n\t"
               "global_load_lds_dwordx4 %2, %4\n\tglobal_load_lds_dwordx4 %3, %4 offset:1024\n\ts_mov_b32 m0, %0"
               : "=&s"(keep) : "s"(lds), "v"(v0), "v"(v1), "s"(sbase) : "memory");
}
DI void dma4s(unsigned lds, const char* s, size_t gstride, unsigned v) {
  unsigned keep;
  asm volatile("s_mov_b32 %0, m0\n\ts_mov_b32 m0, %1\n\ts_nop 3\n\tglobal_load_lds_dwordx4 %2, %3 nt\n\t"
               "s_add_u32 m0, m0, 0x2000\n\ts_nop 0\n\tglobal_load_lds_dwordx4 %2, %4 nt\n\t"
               "s_add_u32 m0, m0, 0x2000\n\ts_nop 0\n\tglobal_load_lds_dwordx4 %2, %5 nt\n\t"
               "s_add_u32 m0, m0, 0x2000\n\ts_nop 0\n\tglobal_load_lds_dwordx4 %2, %6 nt\n\ts_mov_b32 m0, %0"
               : "=&s"(keep) : "s"(lds), "v"(v), "s"(s), "s"(s + gstride), "s"(s + 2 * gstride), "s"(s + 3 * gstride) : "memory", "scc");
}

DI void dma4s_keep(unsigned lds, const char* s, size_t gstride, unsigned v) {
  unsigned keep;
  asm volatile("s_mov_b32 %0, m0\n\ts_mov_b32 m0, %1\n\ts_nop 3\n\tglobal_load_lds_dwordx4 %2, %3\n\t"
               "s_add_u32 m0, m0, 0x400\n\ts_nop 0\n\tglobal_load_lds_dwordx4 %2, %4\n\t"
               "s_add_u32 m0, m0, 0x400\n\ts_nop 0\n\tglobal_load_lds_dwordx4 %2, %5\n\t"
               "s_add_u32 m0, m0, 0x400\n\ts_nop 0\n\tglobal_load_lds_dwordx4 %2, %6\n\ts_mov_b32 m0, %0"
               : "=&s"(keep) : "s"(lds), "v"(v), "s"(s), "s"(s + gstride), "s"(s + 2 * gstride), "s"(s + 3 * gstride) : "memory", "scc");
}

struct Frag { s16x4 lo, hi; };
template <int NF, int NB, int NI, class RD, class MM, class PREV, class MID, class HOOK>
DI void ring_step(Frag (&carry)[RDEF], RD rd, MM mm, PREV prev, MID mid, HOOK hook) {
  static_assert(NI + RDEF <= NB, "");
  Frag fb[NB];
#pragma unroll
  for (int f = 0; f < NI; ++f) { rd(f, 0, fb[f]); rd(f, 1, fb[f]); }
  SBAR();
#pragma unroll
  for (int r = 0; r < RDEF; ++r) {
    const bf16x8 a = cat8(carry[r].lo, carry[r].hi);
    prev(r, 0, a); SBAR(); if (r > 0) { rd(NI + r - 1, 0, fb[NI + r - 1]); SBAR(); }
    prev(r, 1, a); SBAR(); if (r > 0) { rd(NI + r - 1, 1, fb[NI + r - 1]); SBAR(); }
  }
  mid();
  SBAR();
#pragma unroll
  for (int f = NI + RDEF - 1; f < NB - 1; ++f) { rd(f, 0, fb[f]); rd(f, 1, fb[f]); }
  SBAR();
#pragma unroll
  for (int f = 0; f < NF - RDEF; ++f) {
    const bf16x8 a = cat8(fb[f % NB].lo, fb[f % NB].hi);
    const int g = f + NB - 1;
    mm(f, 0, a); SBAR(); if (g < NF) { rd(g, 0, fb[g % NB]); SBAR(); }
    mm(f, 1, a); SBAR(); if (g < NF) { rd(g, 1, fb[g % NB]); SBAR(); }
    hook(f); SBAR();
  }
#pragma unroll
  for (int r = 0; r < RDEF; ++r) carry[r] = fb[(NF - RDEF + r) % NB];
}

__global__ __launch_bounds__(512, 2) void gqa_fused_kernel(
    const char* __restrict__ xA, const char* __restrict__ kA, const char* __restrict__ vA, const char* __restrict__ wqA, const char* __restrict__ woA,
    const char* __restrict__ xB, const char* __restrict__ kB, const char* __restrict__ vB, const char* __restrict__ wqB, const char* __restrict__ woB,
    char* __restrict__ out, char* __restrict__ ws) {
  extern __shared__ __attribute__((aligned(16))) char smem[];
  const unsigned lds0 = (unsigned)(uintptr_t)(lds_ptr)smem;
  int w = __builtin_amdgcn_readfirstlane(threadIdx.x >> 6);
  const int id = blockIdx.x, br = (id >> 2) & 1, kvh = 2 * (id & 3) + ((id >> 3) & 1), b = id >> 4, grp = br * 16 + b;
  int rot7 = b % 7;
  int rg = w & 3, hf = w >> 2;
  unsigned long long pc0; unsigned code_len;
  asm volatile("s_getpc_b64 %0\ngqa_head_label:\n\ts_mov_b32 %1, gqa_tail_label-gqa_head_label" : "=s"(pc0), "=s"(code_len));
  const char* kg = (br ? kB : kA) + (size_t)(b * HKV + kvh) * (SK * DH * 2);
  const char* vg = (br ? vB : vA) + (size_t)(b * HKV + kvh) * (SK * DH * 2);
  const char* wog = (br ? woB : woA) + (size_t)kvh * 512 * (DM * 2);

  auto k_eighth = [&](int t, int e) {
    const int ln = lane_id(), hi = ln >> 5;
    const char* kb = kg; SOPAQUE(kb);
    dma4s(lds0 + (t & 3) * SS + e * 1024, kb + (size_t)t * SS + e * 1024, 8192, hi * 512 + (((ln & 31) ^ (2 * e + hi)) << 4));
  };

  int vrot = (id >> 3) & 3;
  auto issue_vw = [&](int p) {
    const unsigned dst = lds0 + (p & 3) * SS + w * 1024;
    const int ln = lane_id();
    const bool isv = p < 20 || (p < 44 && !(p & 1));
    if (isv) {
      const int n = p < 20 ? p - 16 : 4 + ((p - 20) >> 1);
      const int r8 = ln >> 3;
      const char* vb = vg; SOPAQUE(vb);
      dma4s(dst, vb + (size_t)((n & 3) * 256 + 8 * w) * 512 + (((n >> 2) + vrot) & 3) * 128, 64 * 512, r8 * 512 + (((ln & 7) ^ (r8 & 6)) << 4));
    } else {
      const int k = p < 44 ? (p - 20) >> 1 : p - 32, r8 = ln >> 3;
      const char* wb = wog; SOPAQUE(wb);
      dma4s_keep(lds0 + (p & 3) * SS + w * 4096, wb + (size_t)(((k >> 1) & 1) * 256 + (((k >> 2) + vrot) & 3) * 64 + (k & 1) * 32) * 896 + (w < 7 ? w : 6) * 128,
                 8 * 896, r8 * 896 + (((ln & 7) ^ (r8 & 6)) << 4));
    }
  };
  auto issue = [&](int p) { if (p < 16) k_eighth(p, w); else issue_vw(p); };

  u32x4 pst[16][2];
  f32x4 oa[4][2];
  Frag carry[RDEF];
  float mref[2] = {-INFINITY, -INFINITY};
  float cpk;

  auto vaddr = [&](int slot) {
    const int ln = lane_id(), rl = ((ln >> 4) << 2) | ((ln >> 2) & 3);
    unsigned a = lds0 + slot * SS + hf * 4096 + rl * 128 + ((((ln >> 1) & 1) ^ (rl & 6) ^ (hf << 2)) << 4) + (ln & 1) * 8;
    OPAQUE(a);
    return a;
  };
  auto vu = [](int c, int f) { return c == 0 ? ((f >> 1) & 3) : (f >> 2); };
  auto vd = [](int c, int f) { return c == 0 ? ((f & 1) | ((f & 8) ? 0 : 2)) : (f & 3); };
  auto vrd = [&](auto cc, unsigned vb, int f, int h, Frag& d) {
    constexpr int c = decltype(cc)::value;
    const unsigned a = (vb ^ (unsigned)(vd(c, f) << 5)) + vu(c, f) * 8192;
    if (h == 0) d.lo = ldstr(a); else d.hi = ldstr(a + 2048);
  };
  auto vmm = [&](auto cc, int f, int qb, bf16x8 a) {
    constexpr int c = decltype(cc)::value;
    const int u = vu(c, f), db = vd(c, f);
    const bf16x8 b = __builtin_bit_cast(bf16x8, pst[4 * c + u][qb]);
    if (c == 0 && u == 0) mm1z(oa[db][qb], a, b); else mm1(oa[db][qb], a, b);
  };
  auto nohook = [](int) {};
  auto nop = []() {};

  for (int attempt = 0;; ++attempt) {
  SOPAQUE(w); SOPAQUE(rot7); SOPAQUE(vrot); rg = w & 3; hf = w >> 2;
  if (__builtin_expect(w >= 4, 1)) {
    const int lw = w - 4, lane = lane_id();
    const char* xg = (br ? xB : xA) + (size_t)b * (SQ * DM * 2); SOPAQUE(xg);
    const char* wqg = (br ? wqB : wqA) + (size_t)kvh * 1024; SOPAQUE(wqg);
    unsigned wofs[4];
#pragma unroll
    for (int j = 0; j < 4; ++j) wofs[j] = j * (8192 - 1024) + ((lane ^ (j << 2)) << 4);
    int wsl = 1;
    auto wtile = [&](int i) {
      int G = (i >> 1) + rot7; G = G >= 7 ? G - 7 : G;
      const char* src = wqg + (size_t)(G * 64 + (i & 1) * 32 + 8 * lw) * 8192;
      const unsigned dst = lds0 + wsl * SS + lw * 8192;
      dma4(dst, src, wofs[0], wofs[1], wofs[2], wofs[3]);
      dma4(dst + 4096, src + 4 * 8192, wofs[0], wofs[1], wofs[2], wofs[3]);
      wsl = wsl == 2 ? 0 : wsl + 1;
    };
    const int xr = 16 * lw + (lane >> 3), xc = lane & 7;
    const unsigned x0 = xr * 896 + ((xc ^ ((xr >> 1) & 7)) << 4), x1 = (xr + 8) * 896 + ((xc ^ (((xr + 8) >> 1) & 7)) << 4) - 1024;
    auto xtile = [&](int Gi) { int G = Gi + rot7; G = G >= 7 ? G - 7 : G; dma2(lds0 + XIMG + Gi * 8192 + lw * 2048, xg + G * 128, x0, x1); };
    xtile(0); wtile(0); wtile(1);
    for (int i = 0; i < 14; ++i) {
      if (i == 1) wbar<20>(); else wbar<8>();
      if (i == 0) for (int Gi = 1; Gi < 7; ++Gi) xtile(Gi);
      if (i + 2 < 14) wtile(i + 2); else { k_eighth(i - 12, 2 * lw); k_eighth(i - 12, 2 * lw + 1); }
    }
  } else {
    const int lane = lane_id(), g = lane >> 4, qq = (lane >> 2) & 3, pp = lane & 3;
    const int mh = w & 1, hh = w >> 1, h = lane >> 5, m = lane & 31;
    f32x16 qa[8];
#pragma unroll
    for (int a = 0; a < 8; ++a) qa[a] = (f32x16)(0.f);
    const unsigned wbase = lds0 + (8 * h + qq) * 1024 + hh * 512 + (((2 * (g & 1) + (pp >> 1)) ^ (qq << 2)) << 4) + (pp & 1) * 8;
    const unsigned xbase = lds0 + XIMG + (32 * mh + m) * 128 + ((h ^ ((m >> 1) & 7)) << 4);
    bf16x8 wfA[8], wfB[8], xa, xb, xo, xc = (bf16x8)(0), xd = (bf16x8)(0);
#pragma unroll
    for (int a = 0; a < 8; ++a) wfB[a] = (bf16x8)(0);
    xo = (bf16x8)(0);
    int sl = SS;
    unsigned tch = 0, tch2 = 0;
    for (int i = 0; i < 14; ++i) {
      CBAR();
      if (i == 0 && code_len < 65536u) {
        unsigned o = (w * 64 + lane) * 128; o = o < code_len ? o : 0;
        asm volatile("global_load_dword %0, %1, %2" : "=v"(tch) : "v"(o), "s"(pc0) : "memory");
        o = 32768 + (w * 64 + lane) * 128; o = o < code_len ? o : 0;
        asm volatile("global_load_dword %0, %1, %2" : "=v"(tch2) : "v"(o), "s"(pc0) : "memory");
      }
      unsigned p = wbase + sl; OPAQUE(p);
      sl = sl == 2 * SS ? 0 : sl + SS;
      const unsigned xp = (xbase + (i >> 1) * 8192) ^ (unsigned)((i & 1) << 6);
      if (i < 13) { xa = ldsb128(xp); xb = ldsb128(xp ^ 32); } else { xa = xc; xb = xd; }
      if (i == 12) { xc = ldsb128(xp ^ 64); xd = ldsb128(xp ^ 96); }
      SBAR();
#pragma unroll
      for (int a = 0; a < 8; ++a) {
        const unsigned q = (p ^ (unsigned)((a & 3) << 6)) + (a >> 2) * 256; wfA[a] = cat8(ldstr(q), ldstr(q + 4096));
        qa[a] = MFMA32(wfB[a], xo, qa[a]);
        SBAR();
      }
#pragma unroll
      for (int a = 0; a < 8; ++a) {
        if (a < 4) {
#pragma unroll
          for (int u = 0; u < 2; ++u) { const int a2 = 2 * a + u; const unsigned q = (p ^ (unsigned)((a2 & 3) << 6)) + (a2 >> 2) * 256; wfB[a2] = cat8(ldstr(q + 16384), ldstr(q + 16384 + 4096)); }
        }
        qa[a] = MFMA32(wfA[a], xa, qa[a]);
        SBAR();
      }
      xo = xb;
    }
    asm volatile("s_waitcnt vmcnt(0)" : "+v"(tch), "+v"(tch2) :: "memory");
    const unsigned qw = lds0 + QST + (hh * 2 + mh) * 16384 + (m >> 4) * 8192 + (m & 15) * 16 + h * 8;
#pragma unroll
    for (int nb = 0; nb < 8; ++nb) {
      qa[nb] = MFMA32(wfB[nb], xo, qa[nb]);
#pragma unroll
      for (int a = 0; a < 4; ++a)
        *(LAS u32x2*)(uintptr_t)(qw + nb * 1024 + a * 256) =
            u32x2{cvt2(qa[nb][4 * a] * CEXP, qa[nb][4 * a + 1] * CEXP), cvt2(qa[nb][4 * a + 2] * CEXP, qa[nb][4 * a + 3] * CEXP)};
    }
  }
  CBAR();
  bf16x8 qf[2][8];
  {
    const unsigned qr = lds0 + QST + rg * 16384 + lane_id() * 16;
#pragma unroll
    for (int qb = 0; qb < 2; ++qb)
#pragma unroll
      for (int s = 0; s < 8; ++s) qf[qb][s] = ldsb128(qr + (qb * 8 + s) * 1024);
  }

    f32x4 sa[2][2];
    f32x4 nm[2];
    float tmax[2] = {0.f, 0.f}, lsum[2] = {0.f, 0.f};
    auto kmm = [&](auto tt, int f, int qb, bf16x8 a) {
      constexpr int t = decltype(tt)::value;
      if (f >= 2) mm1(sa[f & 1][qb], a, qf[qb][f >> 1]);
      else if (t == 0) mm1z(sa[f][qb], a, qf[qb][0]);
      else mm1c(sa[f][qb], a, qf[qb][0], nm[qb]);
    };
    auto smax = [&](int qb) {
      return fmaxf(fmaxf(fmaxf(sa[0][qb][0], sa[0][qb][1]), fmaxf(sa[0][qb][2], sa[0][qb][3])), fmaxf(fmaxf(sa[1][qb][0], sa[1][qb][1]), fmaxf(sa[1][qb][2], sa[1][qb][3])));
    };
    auto soft = [&](auto tt) {
      constexpr int t = decltype(tt)::value;
      MFMA_DRAIN();
#pragma unroll
      for (int qb = 0; qb < 2; ++qb) {
        if (t == 0) {
          mref[qb] = fmaxf(mref[qb], xmax(smax(qb)));
          nm[qb] = (f32x4)(-mref[qb]); OPAQUE(nm[qb]);
          sa[0][qb] += nm[qb]; sa[1][qb] += nm[qb];
        } else if (attempt == 1) tmax[qb] = fmaxf(tmax[qb], smax(qb));
        float e[8];
#pragma unroll
        for (int r = 0; r < 8; ++r) e[r] = __builtin_amdgcn_exp2f(sa[r >> 2][qb][r & 3]);
        lsum[qb] += ((e[0] + e[1]) + (e[2] + e[3])) + ((e[4] + e[5]) + (e[6] + e[7])); OPAQUE(lsum[qb]);
        pst[t][qb] = u32x4{cvt2(e[0], e[1]), cvt2(e[2], e[3]), cvt2(e[4], e[5]), cvt2(e[6], e[7])};
        OPAQUE(pst[t][qb]);
      }
    };
    auto kstep = [&](auto tt) {
      constexpr int t = decltype(tt)::value;
      const int ln = lane_id();
      unsigned kb0 = lds0 + (t & 3) * SS + hf * 16384 + (ln & 15) * 512 + (((ln >> 4) ^ (ln & 15)) << 4); OPAQUE(kb0);
      if (t == 0) wbar<8>(); else wbar<4>();
      if (hf == 1) issue(t + 2);
      ring_step<16, NBK, 2>(carry,
          [&](int f, int h, Frag& d) {
            if (h == 0) { const bf16x8 v = ldsb128((kb0 ^ (unsigned)(((f >> 1) & 3) << 6)) + (f >> 3) * 256 + (f & 1) * 8192);
                          d.lo = __builtin_shufflevector(v, v, 0, 1, 2, 3); d.hi = __builtin_shufflevector(v, v, 4, 5, 6, 7); } },
          [&](int f, int qb, bf16x8 a) { kmm(IC<t>{}, f, qb, a); },
          [&](int r, int qb, bf16x8 a) { if constexpr (t > 0) kmm(IC<t - 1>{}, 16 - RDEF + r, qb, a); },
          [&]() { if constexpr (t > 0) soft(IC<t - 1>{}); }, nohook);
      if (hf == 0) issue(t + 2);
    };
    kstep(IC<0>{}); kstep(IC<1>{}); kstep(IC<2>{}); kstep(IC<3>{}); kstep(IC<4>{}); kstep(IC<5>{}); kstep(IC<6>{}); kstep(IC<7>{});
    kstep(IC<8>{}); kstep(IC<9>{}); kstep(IC<10>{}); kstep(IC<11>{}); kstep(IC<12>{}); kstep(IC<13>{}); kstep(IC<14>{}); kstep(IC<15>{});
    {
      const unsigned vb = vaddr(0); wbar<4>(); if (hf == 1) { issue_vw(18); issue_vw(19); }
      ring_step<16, NBV, 2>(carry, [&](int f, int h, Frag& d) { vrd(IC<0>{}, vb, f, h, d); }, [&](int f, int qb, bf16x8 a) { vmm(IC<0>{}, f, qb, a); },
                            [&](int r, int qb, bf16x8 a) { kmm(IC<15>{}, 16 - RDEF + r, qb, a); },
                            [&]() {
                              soft(IC<15>{});
                              const int ln = lane_id();
                              lsum[0] = xsum(lsum[0]); lsum[1] = xsum(lsum[1]);
                              const bool bad = !(lsum[0] <= LMAX) || !(lsum[1] <= LMAX);
                              const float anybad = __any(bad) ? 1.f : 0.f;
                              if (ln < 16) *(LAS f32x4*)(uintptr_t)(lds0 + MISC + w * 256 + ln * 16) = f32x4{mref[0], lsum[0], mref[1], lsum[1]};
                              if (ln == 0) *(LAS float*)(uintptr_t)(lds0 + MISC + 2048 + w * 4) = anybad;
                            }, nohook);
      if (hf == 0) { issue_vw(18); issue_vw(19); }
    }
    float fl;
    {
      const unsigned vb = vaddr(1); wbar<8>(); if (hf == 1) issue_vw(20);
      ring_step<16, NBV, 2>(carry, [&](int f, int h, Frag& d) { vrd(IC<1>{}, vb, f, h, d); }, [&](int f, int qb, bf16x8 a) { vmm(IC<1>{}, f, qb, a); },
                         [&](int r, int qb, bf16x8 a) { vmm(IC<0>{}, 16 - RDEF + r, qb, a); },
                         [&]() {
                           const int ln = lane_id();
                           const f32x4 o = *(const LAS f32x4*)(uintptr_t)(lds0 + MISC + (w ^ 4) * 256 + (ln & 15) * 16);
                           const f32x4 f0 = *(const LAS f32x4*)(uintptr_t)(lds0 + MISC + 2048), f1 = *(const LAS f32x4*)(uintptr_t)(lds0 + MISC + 2048 + 16);
                           fl = (f0[0] + f0[1]) + (f0[2] + f0[3]) + (f1[0] + f1[1]) + (f1[2] + f1[3]); OPAQUE(fl);
                           const bool odd = (ln >> 4) & 1;
                           float l0 = lsum[0], l1 = lsum[1], m0 = mref[0], m1 = mref[1], o0_ = o[0], o1_ = o[1], o2_ = o[2], o3_ = o[3];
                           OPAQUE(l0); OPAQUE(l1); OPAQUE(m0); OPAQUE(m1); OPAQUE(o0_); OPAQUE(o1_); OPAQUE(o2_); OPAQUE(o3_);
                           const float ms = odd ? m1 : m0, ls = odd ? l1 : l0, mo = odd ? o2_ : o0_, lo = odd ? o3_ : o1_;
                           const float m = fmaxf(ms, mo), ea = __builtin_amdgcn_exp2f(ms - m), eb = __builtin_amdgcn_exp2f(mo - m);
                           cpk = ea / (ls * ea + lo * eb); OPAQUE(cpk);
                         }, nohook);
      if (hf == 0) issue_vw(20);
    }
    if (attempt == 2 || (attempt == 0 && __builtin_amdgcn_readfirstlane(__float_as_uint(fl)) == 0u)) break;
    mref[0] += xmax(tmax[0]); mref[1] += xmax(tmax[1]);
    wbar<0>();
  }
  {
    const unsigned vb = vaddr(2); wbar<8>(); if (hf == 1) issue_vw(21);
    ring_step<16, NBV, 2>(carry, [&](int f, int h, Frag& d) { vrd(IC<2>{}, vb, f, h, d); }, [&](int f, int qb, bf16x8 a) { vmm(IC<2>{}, f, qb, a); },
                          [&](int r, int qb, bf16x8 a) { vmm(IC<1>{}, 16 - RDEF + r, qb, a); }, nop, nohook);
    if (hf == 0) issue_vw(21);
  }
  {
    const unsigned vb = vaddr(3); wbar<8>(); if (hf == 1) issue_vw(22);
    ring_step<16, NBV, 2>(carry, [&](int f, int h, Frag& d) { vrd(IC<3>{}, vb, f, h, d); }, [&](int f, int qb, bf16x8 a) { vmm(IC<3>{}, f, qb, a); },
                          [&](int r, int qb, bf16x8 a) { vmm(IC<2>{}, 16 - RDEF + r, qb, a); }, nop, nohook);
  }

  const int qh = w >> 2, nq = w & 3;
  f32x4 pa[7][2];
#pragma unroll
  for (int i = 0; i < 7; ++i) { pa[i][0] = (f32x4)(0.f); pa[i][1] = (f32x4)(0.f); }
  bf16x8 o0 = (bf16x8)(0), o1 = (bf16x8)(0);
  auto oload = [&](int idx) {
    const unsigned oad = lds0 + MISC + ((((idx >> 1) * 2 + qh) + 4 * ((idx & 1) ^ 1)) * 4096) + lane_id() * 16;
    o0 = ldsb128(oad); o1 = ldsb128(oad + 1024);
  };
  auto wmm = [&](int i, int qb, bf16x8 a) { mm1(pa[i][qb], a, qb ? o1 : o0); };
  float c0, c1;
  auto comb_write = [&](int k) {
    if (k == 0) {
      float cc = cpk; OPAQUE(cc);
      const auto cr = __builtin_amdgcn_permlane16_swap(__float_as_uint(cc), __float_as_uint(cc), false, false);
      c0 = __uint_as_float(cr[0]); c1 = __uint_as_float(cr[1]);
    }
    if (k < 2) {
      const unsigned ad = lds0 + MISC + w * 4096 + k * 2048 + lane_id() * 16;
      *(LAS f32x4*)(uintptr_t)(ad) = oa[2 + k][0] * c0; *(LAS f32x4*)(uintptr_t)(ad + 1024) = oa[2 + k][1] * c1;
    } else if (k < 4) { oa[k - 2][0] *= c0; oa[k - 2][1] *= c1; PIN2(oa[k - 2][0], oa[k - 2][1]); }
  };
  f32x4 ct0, ct1;
  auto comb_rd = [&](int qb) {
    const unsigned pad = lds0 + MISC + (w ^ 4) * 4096 + qb * 1024 + lane_id() * 16;
    ct0 = *(const LAS f32x4*)(uintptr_t)(pad); ct1 = *(const LAS f32x4*)(uintptr_t)(pad + 2048);
  };
  auto comb_fin = [&](int qb) {
    const f32x4 a = ct0 + oa[0][qb], c = ct1 + oa[1][qb];
    *(LAS u32x4*)(uintptr_t)(lds0 + MISC + (w ^ 4) * 4096 + qb * 1024 + lane_id() * 16) = u32x4{cvt2(a[0], a[1]), cvt2(a[2], a[3]), cvt2(c[0], c[1]), cvt2(c[2], c[3])};
  };
  auto vstep = [&](int slot, int pn, auto cc) {
    constexpr int c = decltype(cc)::value;
    const unsigned vb = vaddr(slot);
    wbar<8>();
    if (hf == 1) issue_vw(pn);
    ring_step<16, (c == 0 ? NBV - 1 : NBV), 2>(carry, [&](int f, int h, Frag& d) { vrd(cc, vb, f, h, d); }, [&](int f, int qb, bf16x8 a) { vmm(cc, f, qb, a); },
                          [&](int r, int qb, bf16x8 a) { wmm(7 - RDEF + r, qb, a); }, [&]() { if (c == 0) comb_rd(0); },
                          [&](int f) { if (c == 0 && f == 2) { comb_fin(0); comb_rd(1); } if (c == 0 && f == 6) comb_fin(1); });
  };
  auto wstep = [&](int slot, auto pre, auto prev, auto mid, auto hook) {
    const int ln = lane_id(), rl = ((ln >> 4) << 2) | ((ln >> 2) & 3);
    unsigned wb = lds0 + slot * SS + rl * 128 + (((2 * nq + ((ln >> 1) & 1)) ^ (rl & 6)) << 4) + (ln & 1) * 8;
    OPAQUE(wb);
    pre();
    ring_step<7, NBW, NBW - RDEF>(carry, [&](int i, int h, Frag& d) { if (h == 0) d.lo = ldstr(wb + i * 4096); else d.hi = ldstr(wb + i * 4096 + 2048); }, wmm, prev, mid, hook);
  };
#pragma unroll
  for (int r = 0; r < RDEF; ++r) { const bf16x8 a = cat8(carry[r].lo, carry[r].hi); vmm(IC<3>{}, 16 - RDEF + r, 0, a); vmm(IC<3>{}, 16 - RDEF + r, 1, a); carry[r].lo = (s16x4)(0); carry[r].hi = (s16x4)(0); }
  if (hf == 0) issue_vw(22);
  MFMA_DRAIN();
  comb_write(0); comb_write(1); comb_write(2); comb_write(3);
#pragma unroll 1
  for (int p = 20; p < 44; p += 8) {
    vstep(0, p + 3, IC<0>{}); if (hf == 0) issue_vw(p + 3);
    wstep(1, [&]() { wbar<8>(); if (hf == 1) issue_vw(p + 4); oload(0); }, [&](int r, int qb, bf16x8 a) { vmm(IC<0>{}, 16 - RDEF + r, qb, a); }, nop, nohook); if (hf == 0) issue_vw(p + 4);
    vstep(2, p + 5, IC<1>{}); if (hf == 0) issue_vw(p + 5);
    wstep(3, [&]() { wbar<8>(); if (hf == 1) issue_vw(p + 6); oload(1); }, [&](int r, int qb, bf16x8 a) { vmm(IC<1>{}, 16 - RDEF + r, qb, a); }, nop, nohook); if (hf == 0) issue_vw(p + 6);
    vstep(0, p + 7, IC<2>{}); if (hf == 0) issue_vw(p + 7);
    wstep(1, [&]() { wbar<8>(); if (hf == 1) issue_vw(p + 8); oload(2); }, [&](int r, int qb, bf16x8 a) { vmm(IC<2>{}, 16 - RDEF + r, qb, a); }, nop, nohook); if (hf == 0) issue_vw(p + 8);
    vstep(2, p + 9, IC<3>{}); oload(3); if (hf == 0) issue_vw(p + 9);
    wstep(3, [&]() { wbar<8>(); if (hf == 1) issue_vw(p + 10); }, [&](int r, int qb, bf16x8 a) { vmm(IC<3>{}, 16 - RDEF + r, qb, a); }, nop,
          [&](int f) { if (f >= 1) comb_write(f - 1); }); if (hf == 0) issue_vw(p + 10);
  }
  CBAR();
  comb_rd(0);
#pragma unroll
  for (int r = 0; r < RDEF; ++r) { const bf16x8 a = cat8(carry[r].lo, carry[r].hi); wmm(7 - RDEF + r, 0, a); wmm(7 - RDEF + r, 1, a); }
  comb_fin(0); comb_rd(1); issue_vw(47); comb_fin(1);
  unsigned long long* ctl = (unsigned long long*)(ws + (size_t)grp * 2048);
  const __amdgpu_buffer_rsrc_t wsr = __builtin_amdgcn_make_buffer_rsrc(ws, 0, 0x7fffffff, 0x00020000);
  const int lane = lane_id(), g = lane >> 4, li = lane & 15;
  unsigned long long ep;
  {
    const int rl = (g << 2) | ((lane >> 2) & 3);
    unsigned wb = lds0 + rl * 128 + (((2 * nq + ((lane >> 1) & 1)) ^ (rl & 6)) << 4) + (lane & 1) * 8, wb2 = wb + 2 * SS;
    PIN2(wb, wb2);
    wbar<0>();
    ep = __hip_atomic_load(ctl, __ATOMIC_RELAXED, __HIP_MEMORY_SCOPE_AGENT);
    bf16x8 of[4][2];
#pragma unroll
    for (int k = 0; k < 4; ++k) { oload(k); of[k][0] = o0; of[k][1] = o1; }
    auto st = [&](int i) {
      u32x4 f = {cvt2(pa[i][0][0], pa[i][0][1]), cvt2(pa[i][0][2], pa[i][0][3]), cvt2(pa[i][1][0], pa[i][1][1]), cvt2(pa[i][1][2], pa[i][1][3])};
      const unsigned off = (unsigned)WS_PART + (((unsigned)(grp * 56 + w * 7 + i) * 8 + kvh) << 10) + lane * 16;
      __builtin_amdgcn_raw_buffer_store_b128(f, wsr, off, 0, 16);
    };
    ring_step<28 + RDEF, 8, 6>(carry,
        [&](int f, int h, Frag& d) { const int i = (f >> 2) % 7, k = f & 3; const unsigned a = ((k & 2) ? wb2 : wb) + (k & 1) * SS + i * 4096; if (h == 0) d.lo = ldstr(a); else d.hi = ldstr(a + 2048); },
        [&](int f, int qb, bf16x8 a) { mm1(pa[f >> 2][qb], a, of[f & 3][qb]); },
        [&](int, int, bf16x8) {}, nop,
        [&](int f) { if ((f & 3) == 2 && f >= 4) st((f >> 2) - 1); });
    MFMA_DRAIN();
    st(6);
  }
  const unsigned long long tag = ep * 0x9E3779B97F4A7C15ull + 0x632BE59BD9B4E019ull;
  asm volatile("gqa_tail_label:");
  wbar<0>();
  if (w == 0 && lane == 0) __hip_atomic_store(ctl + 16 * (kvh + 1), tag, __ATOMIC_RELAXED, __HIP_MEMORY_SCOPE_AGENT);
  if (w < 7) {
    for (unsigned spin = 0; spin < (1u << 22); ++spin) {
      unsigned long long f = tag;
      if (lane < 8) f = __hip_atomic_load(ctl + 16 * (lane + 1), __ATOMIC_RELAXED, __HIP_MEMORY_SCOPE_AGENT);
      if (__all(f == tag)) break;
      __builtin_amdgcn_s_sleep(2);
    }
    __builtin_amdgcn_fence(__ATOMIC_ACQUIRE, "wavefront");
    const unsigned off = (unsigned)WS_PART + ((unsigned)(grp * 56 + kvh * 7 + w) << 13) + lane * 16;
    u32x4 v[8];
#pragma unroll
    for (int s = 0; s < 8; ++s) v[s] = __builtin_amdgcn_raw_buffer_load_b128(wsr, off + s * 1024, 0, 16);
    float acc[8];
#pragma unroll
    for (int e = 0; e < 8; ++e) acc[e] = 0.f;
#pragma unroll
    for (int s = 0; s < 8; ++s)
#pragma unroll
      for (int e = 0; e < 4; ++e) { acc[2 * e] += __uint_as_float(v[s][e] << 16); acc[2 * e + 1] += __uint_as_float(v[s][e] & 0xffff0000u); }
    const int q = 32 * (kvh >> 2) + li, n = 64 * w + 16 * (kvh & 3) + 4 * g;
    char* o = out + ((size_t)(b * SQ + q) * (2 * DM) + br * DM + n) * 2;
    *(u32x2*)o = u32x2{cvt2(acc[0], acc[1]), cvt2(acc[2], acc[3])};
    *(u32x2*)(o + 16 * 2 * DM * 2) = u32x2{cvt2(acc[4], acc[5]), cvt2(acc[6], acc[7])};
    if (kvh == 0 && w == 0 && lane == 0) __hip_atomic_store(ctl, tag, __ATOMIC_RELAXED, __HIP_MEMORY_SCOPE_AGENT);
  }
}

extern "C" void kernel_launch(void* const* d_in, const int* in_sizes, int n_in, void* d_out, int out_size, void* d_ws, size_t ws_size,
                              hipStream_t stream) {
  (void)in_sizes; (void)n_in; (void)out_size; (void)ws_size;
  (void)hipFuncSetAttribute((const void*)gqa_fused_kernel, hipFuncAttributeMaxDynamicSharedMemorySize, LDS_BYTES);
  gqa_fused_kernel<<<256, 512, LDS_BYTES, stream>>>(
      (const char*)d_in[0], (const char*)d_in[1], (const char*)d_in[2], (const char*)d_in[3], (const char*)d_in[4],
      (const char*)d_in[5], (const char*)d_in[6], (const char*)d_in[7], (const char*)d_in[8], (const char*)d_in[9],
      (char*)d_out, (char*)d_ws);
}
```
